```python
import torch

def _softmax(x, axis=-1):
    return torch.softmax(x, dim=axis)
def _gelu(x):
    return torch.nn.functional.gelu(x, approximate="tanh")
def _silu(x):
    return torch.nn.functional.silu(x)
def _sigmoid(x):
    return torch.sigmoid(x)
def _layernorm(x, axis=-1, eps=1e-5):
    m = x.mean(dim=axis, keepdim=True)
    v = x.var(dim=axis, keepdim=True, unbiased=False)
    return (x - m) / torch.sqrt(v + eps)
def _scatter_add(tgt, idx, src, axis=0):
    return tgt.clone().index_add_(axis, idx, src)
def _segment_sum(src, seg, n):
    out = torch.zeros((n,) + src.shape[1:], dtype=src.dtype)
    return out.index_add_(0, seg, src)
def _pad(x, pw):
    flat = tuple(v for pair in reversed(pw) for v in pair)
    return torch.nn.functional.pad(x, flat)

kernel_name = 'gated_mix__spectral_gfnet__glu_gated_geglu__prompt'

def setup_inputs():

    _w = (lambda a: (lambda t: t.to(torch.bfloat16) if t.is_floating_point() else t)(torch.as_tensor(a)))
    def _A_setup():
        B, S, D = 1, 2048, 448
        Df = D // 2 + 1
        return {
            "x": _w((lambda *s: torch.randn(*s, dtype=torch.bfloat16) * 0.05)(B, S, D)),
            "w_r": _w((lambda *s: torch.randn(*s, dtype=torch.bfloat16) * 0.05)(S, Df) * 0.02),
            "w_i": _w((lambda *s: torch.randn(*s, dtype=torch.bfloat16) * 0.05)(S, Df) * 0.02),
        }
    _A_in = _A_setup()

    def _B_setup():
        B, S, Dm, Df = 1, 2048, 448, 1792
        return {
            "x": _w((lambda *s: torch.randn(*s, dtype=torch.bfloat16) * 0.05)(B, S, Dm)),
            "w_gate": _w((lambda *s: torch.randn(*s, dtype=torch.bfloat16) * 0.2)(Dm, Df) * (Dm ** -0.5)),
            "w_up": _w((lambda *s: torch.randn(*s, dtype=torch.bfloat16) * 0.2)(Dm, Df) * (Dm ** -0.5)),
            "w_down": _w((lambda *s: torch.randn(*s, dtype=torch.bfloat16) * 0.2)(Df, Dm) * (Df ** -0.5)),
        }
    _B_in = _B_setup()

    return {
        "A_x": _A_in["x"],
        "A_w_r": _A_in["w_r"],
        "A_w_i": _A_in["w_i"],
        "B_x": _B_in["x"],
        "B_w_gate": _B_in["w_gate"],
        "B_w_up": _B_in["w_up"],
        "B_w_down": _B_in["w_down"],
        "gate_w": _w((lambda *s: torch.randn(*s, dtype=torch.bfloat16) * 0.05)(448) * 16.0),
    }

def reference(A_x, A_w_r, A_w_i, B_x, B_w_gate, B_w_up, B_w_down, gate_w):
    def _A_ref(x, w_r, w_i):
        B, S, D = x.shape
        X = (lambda x, axes=(-2,-1): torch.fft.rfft2(x.float(), dim=axes))(x, axes=(-2, -1))
        W = w_r + 1j * w_i
        X = X * W
        y = (lambda x, s, axes=(-2,-1): torch.fft.irfft2(x, s=s, dim=axes).to(torch.bfloat16))(X, (S, D), axes=(-2, -1))
        return y

    def _B_ref(x, w_gate, w_up, w_down):
        g = torch.einsum("bsd,df->bsf", x, w_gate)
        u = torch.einsum("bsd,df->bsf", x, w_up)
        h = _gelu(g) * u
        return torch.einsum("bsf,fd->bsd", h, w_down)

    x = A_x
    g = _sigmoid((x * gate_w).sum(-1, keepdims=True))
    a = _A_ref(x, A_w_r, A_w_i)
    b = _B_ref(x, B_w_gate, B_w_up, B_w_down)
    return g * a + (1 - g) * b
```

```cpp
#include <hip/hip_runtime.h>
#include <hip/hip_bf16.h>

typedef unsigned short u16;
typedef unsigned int u32;
typedef __attribute__((ext_vector_type(8))) short bf16x8;
typedef __attribute__((ext_vector_type(4))) short s16x4;
typedef __attribute__((ext_vector_type(4))) float f32x4;
typedef __attribute__((ext_vector_type(2))) float f32x2;
typedef __attribute__((ext_vector_type(16))) float f32x16;
typedef __attribute__((ext_vector_type(4))) unsigned int u32x4;
typedef __attribute__((ext_vector_type(2))) unsigned int u32x2;
typedef __attribute__((ext_vector_type(2))) __bf16 bf16x2;

#if defined(__HIP_DEVICE_COMPILE__)
#define NOPK __attribute__((target("no-packed-fp32-ops")))
#else
#define NOPK
#endif
#define DEV NOPK __device__ __forceinline__

constexpr int S_ = 2048, D_ = 448, F_ = 1792, DF_ = 225;

constexpr double kPi = 3.14159265358979323846264338327950288;
constexpr double tsin(double x) {
  double t = x, s = x;
  for (int i = 1; i < 12; ++i) { t *= -x * x / ((2 * i) * (2 * i + 1)); s += t; }
  return s;
}
constexpr double tcos(double x) {
  double t = 1, s = 1;
  for (int i = 1; i < 12; ++i) { t *= -x * x / ((2 * i - 1) * (2 * i)); s += t; }
  return s;
}
constexpr double cosn(long n, long N) {
  n %= N; if (n < 0) n += N;
  if (2 * n > N) n = N - n;
  if (4 * n > N) return -cosn(N - 2 * n, 2 * N) ;
  if (8 * n > N) return tsin(2 * kPi * (double)(N - 4 * n) / (double)(4 * N));
  return tcos(2 * kPi * (double)n / (double)N);
}
constexpr double sinn(long n, long N) { return cosn(4 * n - N, 4 * N); }

constexpr u16 f2bf(float f) {
  u32 u = __builtin_bit_cast(u32, f);
  return (u16)((u + 0x7FFFu + ((u >> 16) & 1u)) >> 16);
}
constexpr float bf2f(u16 h) { return __builtin_bit_cast(float, (u32)h << 16); }

struct C64 { double c[64], s[64]; };
constexpr C64 mkc64() { C64 t{}; for (int i = 0; i < 64; ++i) { t.c[i] = cosn(i, 64); t.s[i] = sinn(i, 64); } return t; }
constexpr C64 c64 = mkc64();

struct FragTab { u16 v[2][4][2][64][8]; };
constexpr FragTab mkT1() {
  FragTab t{};
  for (int mt = 0; mt < 2; ++mt) for (int ks = 0; ks < 4; ++ks) for (int l = 0; l < 64; ++l) for (int j = 0; j < 8; ++j) {
    int kap = 32 * mt + (l & 31), k1 = kap >> 1, part = kap & 1;
    int n1 = 16 * ks + 8 * (l >> 5) + j;
    double val = part ? -c64.s[(n1 * k1) & 63] : c64.c[(n1 * k1) & 63];
    if (kap == 1) val = (n1 & 1) ? -1.0 : 1.0;
    u16 hi = f2bf((float)val);
    u16 lo = f2bf((float)(val - (double)bf2f(hi)));
    t.v[mt][ks][0][l][j] = hi; t.v[mt][ks][1][l][j] = lo;
  }
  return t;
}
constexpr FragTab mkT3() {
  FragTab t{};
  for (int st = 0; st < 2; ++st) for (int ks = 0; ks < 4; ++ks) for (int l = 0; l < 64; ++l) for (int j = 0; j < 8; ++j) {
    int n1 = 32 * st + (l & 31), kap = 16 * ks + 8 * (l >> 5) + j;
    int k1 = kap >> 1, part = kap & 1;
    double val = part ? -2.0 * c64.s[(n1 * k1) & 63] : 2.0 * c64.c[(n1 * k1) & 63];
    if (k1 == 0) val = part ? ((n1 & 1) ? -1.0 : 1.0) : 1.0;
    val /= (double)(S_ * D_);
    u16 hi = f2bf((float)val);
    u16 lo = f2bf((float)(val - (double)bf2f(hi)));
    t.v[st][ks][0][l][j] = hi; t.v[st][ks][1][l][j] = lo;
  }
  return t;
}
struct Tw2048 { float v[2048][2]; };
struct Tw448 { float v[448][2]; };
constexpr Tw2048 mkTw2048() { Tw2048 t{}; for (int i = 0; i < 2048; ++i) { t.v[i][0] = (float)cosn(i, 2048); t.v[i][1] = (float)-sinn(i, 2048); } return t; }
constexpr Tw448 mkTw448() { Tw448 t{}; for (int i = 0; i < 448; ++i) { t.v[i][0] = (float)cosn(i, 448); t.v[i][1] = (float)-sinn(i, 448); } return t; }

static __constant__ __attribute__((aligned(16))) FragTab gT1 = mkT1();
static __constant__ __attribute__((aligned(16))) FragTab gT3 = mkT3();
static __constant__ __attribute__((aligned(16))) Tw2048 gTw2048 = mkTw2048();
static __constant__ __attribute__((aligned(16))) Tw448 gTw448 = mkTw448();

constexpr float C7_1 = (float)cosn(1, 7), C7_2 = (float)cosn(2, 7), C7_3 = (float)cosn(3, 7);
constexpr float S7_1 = (float)sinn(1, 7), S7_2 = (float)sinn(2, 7), S7_3 = (float)sinn(3, 7);

DEV u32 pk_bf16(float a, float b) { bf16x2 v = {(__bf16)a, (__bf16)b}; return __builtin_bit_cast(u32, v); }
DEV float bflo(u32 v) { return __builtin_bit_cast(float, v << 16); }
DEV float bfhi(u32 v) { return __builtin_bit_cast(float, v & 0xffff0000u); }
DEV float rbf(float a) { return (float)(__bf16)a; }
DEV u32 perm_lo(u32 hi_src, u32 lo_src) { return __builtin_amdgcn_perm(hi_src, lo_src, 0x05040100u); }
DEV u32 perm_hi(u32 hi_src, u32 lo_src) { return __builtin_amdgcn_perm(hi_src, lo_src, 0x07060302u); }
DEV bf16x8 as_frag(u32x4 v) { return __builtin_bit_cast(bf16x8, v); }
DEV f32x4 mfma16(bf16x8 a, bf16x8 b, f32x4 c) { return __builtin_amdgcn_mfma_f32_16x16x32_bf16(a, b, c, 0, 0, 0); }
DEV f32x16 mfma32(bf16x8 a, bf16x8 b, f32x16 c) { return __builtin_amdgcn_mfma_f32_32x32x16_bf16(a, b, c, 0, 0, 0); }

DEV void wg_barrier() {
  __builtin_amdgcn_fence(__ATOMIC_RELEASE, "workgroup");
  __builtin_amdgcn_s_barrier();
  __builtin_amdgcn_fence(__ATOMIC_ACQUIRE, "workgroup");
}

struct cf { float x, y; };
DEV cf cadd(cf a, cf b) { return {a.x + b.x, a.y + b.y}; }
DEV cf csub(cf a, cf b) { return {a.x - b.x, a.y - b.y}; }
DEV cf cmul(cf a, cf b) { return {a.x * b.x - a.y * b.y, a.x * b.y + a.y * b.x}; }
DEV cf cmulc(cf a, cf b) { return {a.x * b.x + a.y * b.y, a.y * b.x - a.x * b.y}; }
template <int INV> DEV cf ctw(cf a, cf w) { return INV ? cmulc(a, w) : cmul(a, w); }
template <int INV> DEV cf rot90(cf a) { return INV ? cf{-a.y, a.x} : cf{a.y, -a.x}; }

template <int INV> DEV void dft4(cf& a0, cf& a1, cf& a2, cf& a3) {
  cf t0 = cadd(a0, a2), t1 = csub(a0, a2), t2 = cadd(a1, a3), t3 = rot90<INV>(csub(a1, a3));
  a0 = cadd(t0, t2); a2 = csub(t0, t2); a1 = cadd(t1, t3); a3 = csub(t1, t3);
}
template <int INV> DEV void dft8(cf (&v)[8]) {
  constexpr float R = 0.70710678118654752440f;
  cf e0 = v[0], e1 = v[2], e2 = v[4], e3 = v[6], o0 = v[1], o1 = v[3], o2 = v[5], o3 = v[7];
  dft4<INV>(e0, e1, e2, e3);
  dft4<INV>(o0, o1, o2, o3);
  cf p1 = INV ? cf{(o1.x - o1.y) * R, (o1.x + o1.y) * R} : cf{(o1.x + o1.y) * R, (o1.y - o1.x) * R};
  cf p2 = rot90<INV>(o2);
  cf p3 = INV ? cf{(-o3.x - o3.y) * R, (o3.x - o3.y) * R} : cf{(o3.y - o3.x) * R, (-o3.x - o3.y) * R};
  v[0] = cadd(e0, o0); v[4] = csub(e0, o0);
  v[1] = cadd(e1, p1); v[5] = csub(e1, p1);
  v[2] = cadd(e2, p2); v[6] = csub(e2, p2);
  v[3] = cadd(e3, p3); v[7] = csub(e3, p3);
}
DEV void dft7_ab(const cf (&z)[7], cf& X0, cf (&A)[3], cf (&B)[3]) {
  cf t1 = cadd(z[1], z[6]), t2 = cadd(z[2], z[5]), t3 = cadd(z[3], z[4]);
  cf s1 = csub(z[1], z[6]), s2 = csub(z[2], z[5]), s3 = csub(z[3], z[4]);
  X0 = {z[0].x + t1.x + t2.x + t3.x, z[0].y + t1.y + t2.y + t3.y};
  A[0] = {z[0].x + C7_1 * t1.x + C7_2 * t2.x + C7_3 * t3.x, z[0].y + C7_1 * t1.y + C7_2 * t2.y + C7_3 * t3.y};
  A[1] = {z[0].x + C7_2 * t1.x + C7_3 * t2.x + C7_1 * t3.x, z[0].y + C7_2 * t1.y + C7_3 * t2.y + C7_1 * t3.y};
  A[2] = {z[0].x + C7_3 * t1.x + C7_1 * t2.x + C7_2 * t3.x, z[0].y + C7_3 * t1.y + C7_1 * t2.y + C7_2 * t3.y};
  B[0] = {S7_1 * s1.x + S7_2 * s2.x + S7_3 * s3.x, S7_1 * s1.y + S7_2 * s2.y + S7_3 * s3.y};
  B[1] = {S7_2 * s1.x - S7_3 * s2.x - S7_1 * s3.x, S7_2 * s1.y - S7_3 * s2.y - S7_1 * s3.y};
  B[2] = {S7_3 * s1.x - S7_1 * s2.x + S7_2 * s3.x, S7_3 * s1.y - S7_1 * s2.y + S7_2 * s3.y};
}

DEV float gelu_mul(float g, float u) {
  const float c1 = -2.0f * 1.4426950408889634f * 0.7978845608028654f;
  const float c2 = c1 * 0.044715f;
  float a = g * __builtin_fmaf(g * g, c2, c1);
  float e = __builtin_amdgcn_exp2f(a);
  float r = __builtin_amdgcn_rcpf(1.0f + e);
  return (g * u) * r;
}

constexpr int OFF_E = 0;
constexpr int OFF_TMO = 256;
constexpr int OFF_HF = 4096;
constexpr int OFF_WF = 8192;
constexpr int OFF_PF = 16384;
constexpr int OFF_RF = 32768;
constexpr int WS_H = 65536;
constexpr int WS_WD = WS_H + S_ * F_ * 2;
constexpr int WS_P = WS_WD + F_ * D_ * 2;
constexpr int WS_R = WS_P + 231 * 2048 * 8;
constexpr int WS_GATE = WS_R + 231 * 2048 * 8;
constexpr int WS_END = WS_GATE + 2048 * 4;

constexpr int L_W = 0;
constexpr int L_BP = 0;
constexpr int L_INV = 65536;
constexpr int L_FA = 114688;
constexpr int L_FB = L_FA + 4 * 4608;
constexpr int L_XP = L_FB + 4 * 4608;
constexpr int L_CTL = L_XP + 8192;
constexpr int L_STAMP = L_CTL + 256;
constexpr int LDS_BYTES = L_STAMP + 1024;
constexpr int C_W = 0;
constexpr int C_G = 4;
constexpr int C_EX = 8;
constexpr int C_U = 16;
constexpr int C_FFT = 20;
constexpr int C_INV = 24;
constexpr int C_B = 28;
constexpr int C_ADONE = 32;
constexpr int C_PCNT = 34;
constexpr int C_RCNT = 35;
constexpr int C_RGO = 36;
constexpr int C_TAG = 40;

#define LAS __attribute__((address_space(3)))
typedef LAS volatile u32 lds_vu32;
typedef LAS volatile u32x4 lds_vu32x4;
typedef unsigned long long u64;
typedef __amdgpu_buffer_rsrc_t rsrc_t;
constexpr int AUX_SC1 = 16;
constexpr int SPIN_LDS = 1 << 18;
constexpr int SPIN_GLB = 1 << 14;

#define STAMP(slot) do {} while (0)
#define STAMP_DUMP() do {} while (0)

#define DRAIN() asm volatile("s_waitcnt vmcnt(0)" ::: "memory")
#define SB0() __builtin_amdgcn_sched_barrier(0)
#define WFENCE() __builtin_amdgcn_fence(__ATOMIC_ACQ_REL, "wavefront")

DEV u32x4 ld128s(rsrc_t rs, int off) { return __builtin_amdgcn_raw_buffer_load_b128(rs, off, 0, AUX_SC1); }
DEV u32x2 ld64s(rsrc_t rs, int off) { return __builtin_amdgcn_raw_buffer_load_b64(rs, off, 0, AUX_SC1); }
DEV u32 ld32s(rsrc_t rs, int off) { return __builtin_amdgcn_raw_buffer_load_b32(rs, off, 0, AUX_SC1); }
DEV void st128s(rsrc_t rs, int off, u32x4 v) { __builtin_amdgcn_raw_buffer_store_b128(v, rs, off, 0, AUX_SC1); }
DEV void st64s(rsrc_t rs, int off, u32x2 v) { __builtin_amdgcn_raw_buffer_store_b64(v, rs, off, 0, AUX_SC1); }
DEV void st32s(rsrc_t rs, int off, u32 v) { __builtin_amdgcn_raw_buffer_store_b32(v, rs, off, 0, AUX_SC1); }
DEV void stc(rsrc_t rs, int off, float x, float y) { st64s(rs, off, u32x2{__builtin_bit_cast(u32, x), __builtin_bit_cast(u32, y)}); }
DEV cf ldc(rsrc_t rs, int off) { f32x2 v = __builtin_bit_cast(f32x2, ld64s(rs, off)); return cf{v[0], v[1]}; }

DEV void lds_set(LAS char* l, int word, u32 v, int lane) {
  __builtin_amdgcn_fence(__ATOMIC_RELEASE, "workgroup");
  if (lane == 0) *(lds_vu32*)(l + L_CTL + word * 4) = v;
}
DEV void lds_wait1(LAS char* l, int word, u32 v) {
  for (int spin = 0; spin < SPIN_LDS; ++spin) {
    u32 f = *(lds_vu32*)(l + L_CTL + word * 4);
    if ((u32)__builtin_amdgcn_readfirstlane(f) >= v) break;
    __builtin_amdgcn_s_sleep(1);
  }
  __builtin_amdgcn_fence(__ATOMIC_ACQUIRE, "workgroup");
}
DEV u32 min4(u32x4 f) { u32 a = f[0] < f[1] ? f[0] : f[1], b = f[2] < f[3] ? f[2] : f[3]; return a < b ? a : b; }
DEV void lds_wait4(LAS char* l, int word, u32 v) {
  for (int spin = 0; spin < SPIN_LDS; ++spin) {
    u32x4 f = *(lds_vu32x4*)(l + L_CTL + word * 4);
    if ((u32)__builtin_amdgcn_readfirstlane(min4(f)) >= v) break;
    __builtin_amdgcn_s_sleep(1);
  }
  __builtin_amdgcn_fence(__ATOMIC_ACQUIRE, "workgroup");
}
DEV void lds_wait8(LAS char* l, int word, u32 v) {
  for (int spin = 0; spin < SPIN_LDS; ++spin) {
    u32x4 f = *(lds_vu32x4*)(l + L_CTL + word * 4);
    u32x4 g = *(lds_vu32x4*)(l + L_CTL + word * 4 + 16);
    u32 a = min4(f), b = min4(g);
    if ((u32)__builtin_amdgcn_readfirstlane(a < b ? a : b) >= v) break;
    __builtin_amdgcn_s_sleep(1);
  }
  __builtin_amdgcn_fence(__ATOMIC_ACQUIRE, "workgroup");
}
DEV u32 lds_arrive(LAS char* l, int word, int lane) {
  __builtin_amdgcn_fence(__ATOMIC_RELEASE, "workgroup");
  u32 old = 0;
  if (lane == 0) old = __hip_atomic_fetch_add((LAS u32*)(l + L_CTL + word * 4), 1u, __ATOMIC_RELAXED, __HIP_MEMORY_SCOPE_WORKGROUP);
  return (u32)__builtin_amdgcn_readfirstlane(old);
}
DEV u32x2 get_tag(LAS char* l) {
  lds_wait1(l, C_TAG + 2, 1u);
  u32 lo = *(lds_vu32*)(l + L_CTL + C_TAG * 4), hi = *(lds_vu32*)(l + L_CTL + C_TAG * 4 + 4);
  return u32x2{(u32)__builtin_amdgcn_readfirstlane(lo), (u32)__builtin_amdgcn_readfirstlane(hi)};
}
DEV void put_tag(LAS char* l, u32x2 ep, int w, int lane) {
  if (w == 7) {
    u64 tg = (((u64)ep[1] << 32) | ep[0]) + 1ull;
    if (lane == 0) { *(lds_vu32*)(l + L_CTL + C_TAG * 4) = (u32)tg; *(lds_vu32*)(l + L_CTL + C_TAG * 4 + 4) = (u32)(tg >> 32); }
    lds_set(l, C_TAG + 2, 1u, lane);
  }
}
DEV bool tag_ok2(u32x4 v, u32x2 tag) { return v[0] == tag[0] && v[1] == tag[1] && v[2] == tag[0] && v[3] == tag[1]; }
DEV void poll256(rsrc_t rs, int off, u32x2 tag, int lane) {
  for (int spin = 0; spin < SPIN_GLB; ++spin) {
    u32x4 a = ld128s(rs, off + lane * 16), b = ld128s(rs, off + 1024 + lane * 16);
    bool ok = tag_ok2(a, tag) && tag_ok2(b, tag);
    if (__builtin_amdgcn_ballot_w64(!ok) == 0ull) { __builtin_amdgcn_fence(__ATOMIC_ACQUIRE, "wavefront"); return; }
    __builtin_amdgcn_s_sleep(2);
  }
  if (lane == 0) st32s(rs, OFF_TMO, 1u);
}
DEV bool flag_ok(u32x2 v, u32x2 tag, bool active) { return __builtin_amdgcn_ballot_w64(active && !(v[0] == tag[0] && v[1] == tag[1])) == 0ull; }
DEV void poll_lanes(rsrc_t rs, int off, bool active, u32x2 tag, int lane) {
  for (int spin = 0; spin < SPIN_GLB; ++spin) {
    u32x2 v = ld64s(rs, off);
    if (flag_ok(v, tag, active)) { __builtin_amdgcn_fence(__ATOMIC_ACQUIRE, "wavefront"); return; }
    __builtin_amdgcn_s_sleep(2);
  }
  if (lane == 0) st32s(rs, OFF_TMO, 2u);
}

DEV void dft_piece(const u16* __restrict__ x, LAS char* lds3, rsrc_t rs, int pn2, int pj, int tb, int lane) {
  const int mt = tb >> 1, nt = tb & 1, hh = lane >> 5;
  bf16x8 af[4][2];
  u32x2 ep;
  {
    const int t2 = tb * 64 + lane;
    const int p0 = t2, p1 = t2 < 192 ? t2 + 256 : t2;
    const int r0 = p0 / 7, a0 = p0 % 7, r1 = p1 / 7, a1 = p1 % 7;
    u32x4 g0 = *(const u32x4*)(x + (size_t)(32 * r0 + pn2) * D_ + 64 * a0 + 8 * pj);
    u32x4 g1 = *(const u32x4*)(x + (size_t)(32 * r1 + pn2) * D_ + 64 * a1 + 8 * pj);
#pragma unroll
    for (int ks = 0; ks < 4; ++ks)
#pragma unroll
      for (int hl = 0; hl < 2; ++hl) af[ks][hl] = *(const bf16x8*)&gT1.v[mt][ks][hl][lane][0];
    ep = ld64s(rs, tb == 3 ? OFF_E : (int)0x7ff00000);
    SB0();
    *(LAS u32x4*)(lds3 + L_XP + r0 * 128 + ((a0 * 16) ^ ((r0 & 2) << 5))) = g0;
    *(LAS u32x4*)(lds3 + L_XP + r1 * 128 + ((a1 * 16) ^ ((r1 & 2) << 5))) = g1;
  }
  lds_set(lds3, C_G + tb, 1u, lane);
  lds_wait4(lds3, C_G, 1u);
  STAMP(12);
  {
    const int cq = 4 * ((lane >> 4) & 1) + (lane & 3), qq = (lane & 15) >> 2;
    int m1 = 4 * nt + (cq >> 1); m1 = m1 > 6 ? 6 : m1;
    constexpr int XS = 128;
    LAS char* tbp = lds3 + L_XP + (8 * hh + qq) * XS + ((m1 * 16) ^ ((qq & 2) << 5)) + 8 * (cq & 1);
    f32x16 acc;
#pragma unroll
    for (int i = 0; i < 16; ++i) acc[i] = 0.f;
#pragma unroll
    for (int ks = 0; ks < 4; ++ks) {
      s16x4 lo = __builtin_amdgcn_ds_read_tr16_b64_v4i16((LAS s16x4*)(tbp + 16 * ks * XS));
      s16x4 hi = __builtin_amdgcn_ds_read_tr16_b64_v4i16((LAS s16x4*)(tbp + (16 * ks + 4) * XS));
      bf16x8 b = __builtin_shufflevector(lo, hi, 0, 1, 2, 3, 4, 5, 6, 7);
      acc = mfma32(af[ks][1], b, acc);
      acc = mfma32(af[ks][0], b, acc);
    }
    const int col = lane & 31, cm1 = 4 * nt + (col >> 3);
    if (cm1 < 7) {
#pragma unroll
      for (int p = 0; p < 8; ++p) {
        const int k1 = 16 * mt + 4 * (p >> 1) + 2 * hh + (p & 1);
        *(LAS f32x2*)(lds3 + L_FB + ((k1 * 7 + cm1) * 8 + (col & 7)) * 8) = f32x2{acc[2 * p], acc[2 * p + 1]};
      }
    }
  }
  lds_set(lds3, C_U + tb, 1u, lane);
  put_tag(lds3, ep, tb + 4, lane);
  lds_wait4(lds3, C_U, 1u);
  STAMP(13);
  {
    const int t2 = tb * 64 + lane, k1 = t2 >> 3, e = t2 & 7;
    cf z[7];
#pragma unroll
    for (int m1 = 0; m1 < 7; ++m1) { f32x2 v = *(LAS f32x2*)(lds3 + L_FB + ((k1 * 7 + m1) * 8 + e) * 8); z[m1] = {v[0], v[1]}; }
    cf X0, A[3], B[3];
    dft7_ab(z, X0, A, B);
    const int o0 = WS_P + ((pn2 * 8 + pj) * 7 * 33 * 8 + k1 * 8 + e) * 8;
    constexpr int IS = 33 * 64;
    if (k1 == 0) {
      const int o32 = o0 + 32 * 64;
      stc(rs, o0, X0.x, 0.f);
      stc(rs, o32, X0.y, 0.f);
#pragma unroll
      for (int l = 0; l < 3; ++l) {
        stc(rs, o0 + (l + 1) * IS, A[l].x, -B[l].x);
        stc(rs, o0 + (6 - l) * IS, A[l].x, B[l].x);
        stc(rs, o32 + (l + 1) * IS, A[l].y, -B[l].y);
        stc(rs, o32 + (6 - l) * IS, A[l].y, B[l].y);
      }
    } else {
      stc(rs, o0, X0.x, X0.y);
#pragma unroll
      for (int l = 0; l < 3; ++l) {
        stc(rs, o0 + (l + 1) * IS, A[l].x + B[l].y, A[l].y - B[l].x);
        stc(rs, o0 + (6 - l) * IS, A[l].x - B[l].y, A[l].y + B[l].x);
      }
    }
  }
  STAMP(14);
  DRAIN();
  STAMP(15);
  if (lds_arrive(lds3, C_PCNT, lane) == 3u) {
    u32x2 tag = get_tag(lds3);
    if (lane < 8) st64s(rs, OFF_PF + lane * 2048 + (pn2 * 8 + pj) * 8, tag);
  }
}

DEV void glds16(const char* sbase, u32 voff, u32 lds_dst) {
  u32 keep;
  asm volatile("s_mov_b32 %0, m0\n\ts_mov_b32 m0, %3\n\ts_nop 0\n\tglobal_load_lds_dwordx4 %1, %2\n\ts_mov_b32 m0, %0"
               : "=&s"(keep) : "v"(voff), "s"(sbase), "s"(lds_dst) : "memory");
}
template <int OFF> DEV u32x4 gld16_asm(const char* sbase, u32 voff) {
  u32x4 v;
  asm volatile("global_load_dwordx4 %0, %1, %2 offset:%3" : "=v"(v) : "v"(voff), "s"(sbase), "i"(OFF) : "memory");
  return v;
}
template <int N> DEV void wait_x4(u32x4& a, u32x4& b, u32x4& c, u32x4& d) {
  asm volatile("s_waitcnt vmcnt(%4)" : "+v"(a), "+v"(b), "+v"(c), "+v"(d) : "i"(N) : "memory");
}

constexpr int PD = 3;
template <int C> struct IC { static constexpr int v = C; };
template <bool TA>
DEV void ffn1_kloop(const u16* __restrict__ x, const u16* __restrict__ wg, const u16* __restrict__ wu, int T, int fb,
                    LAS char* lds3, int w, int lane, f32x4 (&acc)[2][4][2]) {
  const int wl = w & 3;
  const char* xb = (const char*)(x + (size_t)(1024 * (T & 1) + (T >> 1)) * D_);
  const u32 xvo = (u32)((lane >> 3) * 32 * D_ * 2 + (lane & 7) * 16);
  constexpr u32 XI = 8 * 32 * D_ * 2;
  u32x4 xs[7][4];
  const char* wb = (const char*)(((wl & 1) ? wu : wg) + 64 * fb + (size_t)((wl >> 1) * 32) * F_);
  const int pp = lane & 7, b1 = (lane >> 3) & 3;
  const u32 wv0 = (u32)((lane >> 3) * F_ * 2 + ((((pp >> 1) ^ b1) * 2 + (pp & 1)) * 16));
  const u32 wv1 = (u32)(((lane >> 3) + 8) * F_ * 2 + ((((pp >> 1) ^ b1 ^ 1) * 2 + (pp & 1)) * 16));
  const u32 ld0 = (u32)(size_t)lds3 + L_W + (wl & 1) * 8192 + (wl >> 1) * 4096;
#define CH(c) do { if (TA) { \
      glds16(wb + (size_t)((c) * 64) * F_ * 2, wv0, ld0 + (c) * 16384); \
      glds16(wb + (size_t)((c) * 64) * F_ * 2, wv1, ld0 + (c) * 16384 + 1024); \
      glds16(wb + (size_t)((c) * 64 + 16) * F_ * 2, wv0, ld0 + (c) * 16384 + 2048); \
      glds16(wb + (size_t)((c) * 64 + 16) * F_ * 2, wv1, ld0 + (c) * 16384 + 3072); \
      xs[c][0] = gld16_asm<(c) * 128>(xb, xvo); \
      xs[c][1] = gld16_asm<(c) * 128>(xb, xvo + XI); \
      xs[c][2] = gld16_asm<(c) * 128>(xb, xvo + 2 * XI); \
      xs[c][3] = gld16_asm<(c) * 128>(xb, xvo + 3 * XI); \
    } else { \
      _Pragma("unroll") for (int i = 0; i < 4; ++i) xs[c][i] = *(const u32x4*)(xb + (c) * 128 + (xvo + i * XI)); \
    } SB0(); } while (0)
  CH(0); CH(1); CH(2);
  STAMP(2);
#pragma unroll
  for (int a = 0; a < 2; ++a)
#pragma unroll
    for (int b = 0; b < 4; ++b) { acc[a][b][0] = f32x4{0.f, 0.f, 0.f, 0.f}; acc[a][b][1] = f32x4{0.f, 0.f, 0.f, 0.f}; }
  LAS char* rl = lds3 + (TA ? L_FA : L_FB) + wl * 4608;
  const int wr0 = lane >> 3;
  LAS char* rw_e = rl + wr0 * 128 + (((lane & 7) ^ (wr0 >> 1)) * 16);
  LAS char* rw_o = rl + wr0 * 128 + (((lane & 7) ^ ((wr0 >> 1) | 4)) * 16);
  const int c16 = lane & 15, q = lane >> 4;
  LAS char* rr0 = rl + c16 * 128 + ((q ^ (c16 >> 1)) * 16);
  LAS char* rr1 = rl + c16 * 128 + (((q | 4) ^ (c16 >> 1)) * 16);
  const int qq = (lane & 15) >> 2, p4 = lane & 3;
  const int fl = qq ^ (q & 1);
  LAS char* wt[4];
#pragma unroll
  for (int t = 0; t < 4; ++t) wt[t] = lds3 + L_W + (8 * q + qq) * 128 + ((t ^ fl) * 32) + 8 * p4;
#define TRD(tau, off) __builtin_shufflevector( \
      __builtin_amdgcn_ds_read_tr16_b64_v4i16((LAS s16x4*)(wt[tau] + (off))), \
      __builtin_amdgcn_ds_read_tr16_b64_v4i16((LAS s16x4*)(wt[tau] + (off) + 512)), 0, 1, 2, 3, 4, 5, 6, 7)
#define GOFF(G) (((G) >> 2) * 16384 + ((G) & 1) * 8192 + (((G) >> 1) & 1) * 4096)
#define LDA(G) do { _Pragma("unroll") for (int t = 0; t < 4; ++t) afr[(G) % 3][t] = TRD(t, GOFF(G)); } while (0)
#define RELAY(c) do { \
      WFENCE(); \
      *(LAS u32x4*)(rw_e) = xs[c][0]; *(LAS u32x4*)(rw_o + 1024) = xs[c][1]; \
      *(LAS u32x4*)(rw_e + 2048) = xs[c][2]; *(LAS u32x4*)(rw_o + 3072) = xs[c][3]; \
      WFENCE(); \
      bfr[(c) & 1][0][0] = *(LAS bf16x8*)(rr0); bfr[(c) & 1][0][1] = *(LAS bf16x8*)(rr0 + 2048); \
      bfr[(c) & 1][1][0] = *(LAS bf16x8*)(rr1); bfr[(c) & 1][1][1] = *(LAS bf16x8*)(rr1 + 2048); } while (0)
#define LAND(c, N) do { if (TA) { wait_x4<N>(xs[c][0], xs[c][1], xs[c][2], xs[c][3]); \
      if (lane == 0) *(lds_vu32*)(lds3 + L_CTL + (C_W + wl) * 4) = (u32)((c) + 1); } } while (0)
#define MMG(G) do { _Pragma("unroll") for (int t = 0; t < 4; ++t) { \
        acc[(G) & 1][t][0] = mfma16(afr[(G) % 3][t], bfr[((G) >> 2) & 1][((G) >> 1) & 1][0], acc[(G) & 1][t][0]); \
        acc[(G) & 1][t][1] = mfma16(afr[(G) % 3][t], bfr[((G) >> 2) & 1][((G) >> 1) & 1][1], acc[(G) & 1][t][1]); } } while (0)
#define ILV() do { _Pragma("unroll") for (int i = 0; i < 4; ++i) { \
        __builtin_amdgcn_sched_group_barrier(0x008, 2, 0); __builtin_amdgcn_sched_group_barrier(0x100, 2, 0); } SB0(); } while (0)
  bf16x8 bfr[2][2][2];
  bf16x8 afr[3][4];
  if (!TA) lds_wait1(lds3, C_PCNT, 4u);
  LAND(0, 16);
  RELAY(0);
  lds_wait4(lds3, C_W, 1u);
  STAMP(3);
  LDA(0); LDA(1);
  SB0();
#define CHUNK(c, NW) do { \
    if ((c) + PD < 7) CH((c) + PD < 7 ? (c) + PD : 6); \
    LDA(4 * (c) + 2); MMG(4 * (c)); ILV(); \
    u32x4 flg = {0u, 0u, 0u, 0u}; \
    if ((c) + 1 < 7) { LAND((c) + 1 < 7 ? (c) + 1 : 6, NW); RELAY((c) + 1 < 7 ? (c) + 1 : 6); flg = *(lds_vu32x4*)(lds3 + L_CTL + C_W * 4); } \
    SB0(); \
    LDA(4 * (c) + 3); MMG(4 * (c) + 1); ILV(); \
    if ((c) + 1 < 7) { \
      if ((u32)__builtin_amdgcn_readfirstlane(min4(flg)) < (u32)((c) + 2)) lds_wait4(lds3, C_W, (u32)((c) + 2)); \
      __builtin_amdgcn_fence(__ATOMIC_ACQUIRE, "workgroup"); \
      SB0(); \
      LDA(4 * (c) + 4); MMG(4 * (c) + 2); ILV(); \
      LDA(4 * (c) + 5); MMG(4 * (c) + 3); ILV(); \
    } else { MMG(4 * (c) + 2); SB0(); MMG(4 * (c) + 3); SB0(); } \
  } while (0)
  CHUNK(0, 16); CHUNK(1, 16); CHUNK(2, 16); CHUNK(3, 16); CHUNK(4, 8); CHUNK(5, 0); CHUNK(6, 0);
#undef CHUNK
#undef CH
#undef TRD
#undef GOFF
#undef LDA
#undef RELAY
#undef LAND
#undef MMG
#undef ILV
}

DEV void ffn1_epilogue(rsrc_t rs, int T, int fb, int lane, f32x4 (&acc)[2][4][2]) {
  const int c16 = lane & 15, q = lane >> 4;
#pragma unroll
  for (int pi = 0; pi < 2; ++pi)
#pragma unroll
    for (int z = 0; z < 2; ++z) {
      u32x4 hv;
#pragma unroll
      for (int tt = 0; tt < 2; ++tt) {
        f32x4 g = acc[0][2 * pi + tt][z], u = acc[1][2 * pi + tt][z];
        hv[2 * tt] = pk_bf16(gelu_mul(g[0], u[0]), gelu_mul(g[1], u[1]));
        hv[2 * tt + 1] = pk_bf16(gelu_mul(g[2], u[2]), gelu_mul(g[3], u[3]));
      }
      st128s(rs, WS_H + ((T * 112 + 4 * fb + 2 * pi + (q >> 1)) * 64 + (q & 1) * 32 + 16 * z + c16) * 16, hv);
    }
}

DEV void aux_phase(const u16* __restrict__ x, const u16* __restrict__ wd, const u16* __restrict__ gw, rsrc_t rs,
                   int a, LAS char* lds3, int w, int lane) {
  const int tid = w * 64 + lane;
  const int lc = lane < 56 ? lane : 0;
  {
    const int id0 = tid, id1 = tid < 136 ? tid + 256 : tid;
    u32x4 rw[2][8];
#pragma unroll
    for (int it = 0; it < 2; ++it) {
      const int id = it ? id1 : id0;
      const int g = 7 * a + id / 56, seg = id % 56;
      const u16* src = wd + (size_t)(32 * (g >> 2) + 4 * (g & 3)) * D_ + seg * 8;
#pragma unroll
      for (int j = 0; j < 8; ++j) rw[it][j] = *(const u32x4*)(src + (size_t)((j & 3) + 16 * (j >> 2)) * D_);
    }
    SB0();
#pragma unroll
    for (int it = 0; it < 2; ++it) {
      const int id = it ? id1 : id0;
      const int grp = id / 56, seg = id % 56;
      const int m1 = seg >> 3, j = seg & 7;
      const int rho0 = 16 * ((m1 >> 1) & 1) + 4 * (m1 & 1);
      LAS char* stg = lds3 + L_W + (grp * 8 + j) * 896;
#pragma unroll
      for (int e2 = 0; e2 < 4; ++e2) {
        u32x4 o0, o1;
#pragma unroll
        for (int p = 0; p < 4; ++p) {
          o0[p] = perm_lo(rw[it][2 * p + 1][e2], rw[it][2 * p][e2]);
          o1[p] = perm_hi(rw[it][2 * p + 1][e2], rw[it][2 * p][e2]);
        }
        const int rho = rho0 + 8 * (e2 >> 1) + 2 * (e2 & 1);
        const int pos = (m1 < 4) ? rho : (32 + (rho < 20 ? rho : rho - 4));
        *(LAS u32x4*)(stg + pos * 16) = o0;
        *(LAS u32x4*)(stg + pos * 16 + 16) = o1;
      }
    }
  }
  lds_set(lds3, C_EX + w, 1u, lane);
  u32x4 xv[16];
#pragma unroll
  for (int i = 0; i < 16; ++i) xv[i] = *(const u32x4*)(x + (size_t)(32 * (16 * w + i) + a) * D_ + lc * 8);
  u32x4 gv = *(const u32x4*)(gw + lc * 8);
  SB0();
  STAMP(2);
  lds_wait4(lds3, C_EX, 1u);
  if (lane < 56) {
#pragma unroll
    for (int i = 0; i < 14; ++i) {
      const int pr = w + 4 * i, grp = pr >> 3, j = pr & 7, g = 7 * a + grp, hh = g & 1;
      u32x4 v = *(LAS u32x4*)(lds3 + L_W + pr * 896 + lane * 16);
      const int pos = lane < 32 ? hh * 32 + lane : 64 + hh * 24 + (lane - 32);
      st128s(rs, WS_WD + (j * 112 + (g >> 1)) * 1792 + pos * 16, v);
    }
  }
  STAMP(3);
  float gwf[8];
#pragma unroll
  for (int p = 0; p < 4; ++p) { gwf[2 * p] = lane < 56 ? bflo(gv[p]) : 0.f; gwf[2 * p + 1] = lane < 56 ? bfhi(gv[p]) : 0.f; }
  LAS char* sc = lds3 + L_FA + w * 4608;
#pragma unroll
  for (int i = 0; i < 16; ++i) {
    float sacc = 0.f;
#pragma unroll
    for (int p = 0; p < 4; ++p) {
      u32 pr = pk_bf16(bflo(xv[i][p]) * gwf[2 * p], bfhi(xv[i][p]) * gwf[2 * p + 1]);
      sacc += bflo(pr);
      sacc += bfhi(pr);
    }
    *(LAS float*)(sc + i * 272 + lane * 4) = sacc;
  }
  WFENCE();
  float tot = 0.f;
#pragma unroll
  for (int k = 0; k < 4; ++k) {
    f32x4 v = *(LAS f32x4*)(sc + (lane & 15) * 272 + (lane >> 4) * 64 + k * 16);
    tot += (v[0] + v[1]) + (v[2] + v[3]);
  }
  tot += __builtin_bit_cast(float, __builtin_amdgcn_ds_bpermute((lane ^ 16) << 2, __builtin_bit_cast(int, tot)));
  tot += __builtin_bit_cast(float, __builtin_amdgcn_ds_bpermute((lane ^ 32) << 2, __builtin_bit_cast(int, tot)));
  if (lane < 16) {
    float z = rbf(tot);
    float g = __builtin_amdgcn_rcpf(1.0f + __builtin_amdgcn_exp2f(-1.4426950408889634f * z));
    st32s(rs, WS_GATE + (a * 64 + 16 * w + lane) * 4, __builtin_bit_cast(u32, rbf(g)));
  }
}

DEV cf ldtw2048(int i) { return cf{gTw2048.v[i][0], gTw2048.v[i][1]}; }
DEV cf ldtw448(int i) { return cf{gTw448.v[i][0], gTw448.v[i][1]}; }
DEV void lst(LAS char* p, cf v) { *(LAS f32x2*)p = f32x2{v.x, v.y}; }
DEV cf lld(LAS char* p) { f32x2 t = *(LAS f32x2*)p; return cf{t[0], t[1]}; }

struct FftPre { u32 wre[8], wim[8], wx[4]; cf twb[8]; };
DEV void fft_prefetch(FftPre& f, const u16* __restrict__ w_r, const u16* __restrict__ w_i, int item, int w, int lane) {
  const int k1 = item / 7, l1 = item % 7;
  const int row = lane >> 3, lo3 = lane & 7;
  const int k2 = 2 * w + (row >> 2) + 8 * (row & 3);
  const int ks = k1 + 64 * k2, nks = (S_ - ks) & (S_ - 1);
#pragma unroll
  for (int be = 0; be < 8; ++be) {
    const int kd = l1 + 7 * lo3 + 56 * be;
    const int idx = kd <= 224 ? ks * DF_ + kd : nks * DF_ + (D_ - kd);
    f.wre[be] = w_r[idx];
    f.wim[be] = w_i[idx];
  }
  f.wx[0] = w_r[nks * DF_]; f.wx[1] = w_i[nks * DF_]; f.wx[2] = w_r[nks * DF_ + 224]; f.wx[3] = w_i[nks * DF_ + 224];
#pragma unroll
  for (int al = 0; al < 8; ++al) f.twb[al] = ldtw448(lo3 * (7 * al + l1));
}
DEV void fft_loadP(cf (&v)[8], rsrc_t rs, int item, int w, int lane) {
  const int k1 = item / 7, l1 = item % 7;
#pragma unroll
  for (int c = 0; c < 8; ++c) v[c] = ldc(rs, WS_P + ((((4 * c + w) * 8 + (lane >> 3)) * 7 + l1) * 33 * 8 + k1 * 8 + (lane & 7)) * 8);
}

DEV void fft_item(rsrc_t rs, const FftPre& f, cf (&v)[8], int item, LAS char* lds3, int w, int lane) {
  const int k1 = item / 7, l1 = item % 7;
  LAS char* base = lds3 + L_FA;
  LAS char* mine = base + w * 4608;
  const int row = lane >> 3, lo3 = lane & 7;
#pragma unroll
  for (int c = 1; c < 8; ++c) v[c] = cmul(v[c], ldtw2048(4 * c * k1));
  dft8<0>(v);
#pragma unroll
  for (int g = 0; g < 8; ++g) v[g] = cmul(v[g], ldtw2048(w * (k1 + 64 * g)));
  lds_wait1(lds3, C_ADONE, 4u);
#pragma unroll
  for (int g = 0; g < 8; ++g) lst(base + (g >> 1) * 4608 + (((g & 1) * 4 + w) * 64 + lane) * 8, v[g]);
  lds_set(lds3, C_FFT + w, 1u, lane);
  STAMP(8);
  lds_wait4(lds3, C_FFT, 1u);
#pragma unroll
  for (int i = 0; i < 8; ++i) v[i] = lld(mine + (i * 64 + lane) * 8);
  dft4<0>(v[0], v[1], v[2], v[3]);
  dft4<0>(v[4], v[5], v[6], v[7]);
  WFENCE();
#pragma unroll
  for (int i = 0; i < 8; ++i) lst(mine + i * 576 + lane * 8, v[i]);
  WFENCE();
#pragma unroll
  for (int a = 0; a < 8; ++a) v[a] = lld(mine + row * 576 + (8 * a + lo3) * 8);
#pragma unroll
  for (int a = 1; a < 8; ++a) v[a] = cmul(v[a], ldtw448(8 * a * l1));
  dft8<0>(v);
#pragma unroll
  for (int al = 0; al < 8; ++al) v[al] = cmul(v[al], f.twb[al]);
  WFENCE();
#pragma unroll
  for (int al = 0; al < 8; ++al) lst(mine + row * 576 + al * 72 + lo3 * 8, v[al]);
  WFENCE();
#pragma unroll
  for (int bb = 0; bb < 8; ++bb) v[bb] = lld(mine + row * 576 + lo3 * 72 + bb * 8);
  dft8<0>(v);
#pragma unroll
  for (int be = 0; be < 8; ++be) {
    const int kd = l1 + 7 * lo3 + 56 * be;
    cf wf = {bflo(f.wre[be]), bflo(f.wim[be])};
    if (kd > 224) wf.y = -wf.y;
    if (be == 0 && kd == 0) wf = {0.5f * (wf.x + bflo(f.wx[0])), 0.5f * (wf.y - bflo(f.wx[1]))};
    if (be == 4 && kd == 224) wf = {0.5f * (wf.x + bflo(f.wx[2])), 0.5f * (wf.y - bflo(f.wx[3]))};
    v[be] = cmul(v[be], wf);
  }
  dft8<1>(v);
  WFENCE();
#pragma unroll
  for (int bb = 0; bb < 8; ++bb) lst(mine + row * 576 + lo3 * 72 + bb * 8, v[bb]);
  WFENCE();
#pragma unroll
  for (int al = 0; al < 8; ++al) v[al] = lld(mine + row * 576 + al * 72 + lo3 * 8);
#pragma unroll
  for (int al = 0; al < 8; ++al) v[al] = cmulc(v[al], f.twb[al]);
  dft8<1>(v);
#pragma unroll
  for (int a = 1; a < 8; ++a) v[a] = cmulc(v[a], ldtw448(8 * a * l1));
  WFENCE();
#pragma unroll
  for (int a = 0; a < 8; ++a) lst(mine + row * 576 + (8 * a + lo3) * 8, v[a]);
  WFENCE();
#pragma unroll
  for (int i = 0; i < 8; ++i) v[i] = lld(mine + i * 576 + lane * 8);
  dft4<1>(v[0], v[1], v[2], v[3]);
  dft4<1>(v[4], v[5], v[6], v[7]);
  WFENCE();
#pragma unroll
  for (int i = 0; i < 8; ++i) lst(mine + (i * 64 + lane) * 8, v[i]);
  lds_set(lds3, C_FFT + w, 2u, lane);
  lds_wait4(lds3, C_FFT, 2u);
#pragma unroll
  for (int g = 0; g < 8; ++g) v[g] = lld(base + (g >> 1) * 4608 + (((g & 1) * 4 + w) * 64 + lane) * 8);
#pragma unroll
  for (int g = 0; g < 8; ++g) v[g] = cmulc(v[g], ldtw2048(w * (k1 + 64 * g)));
  dft8<1>(v);
#pragma unroll
  for (int c = 1; c < 8; ++c) v[c] = cmulc(v[c], ldtw2048(4 * c * k1));
#pragma unroll
  for (int c = 0; c < 8; ++c) stc(rs, WS_R + ((item * 32 + 4 * c + w) * 64 + lane) * 8, v[c].x, v[c].y);
}

DEV void final_phase(rsrc_t rs, u16* __restrict__ out, int n2, int j, LAS char* lds3, int w, int lane) {
  const int tid = w * 64 + lane;
  const int t = w >> 1, st = w & 1, r = lane & 31, hh = lane >> 5;
  const int k1 = tid >> 3, e = tid & 7;
  const int ro = WS_R + ((k1 * 7 * 32 + n2) * 64 + 8 * j + e) * 8;
  cf z[7];
#pragma unroll
  for (int l = 0; l < 7; ++l) z[l] = ldc(rs, ro + l * 16384);
  if (k1 == 0) {
#pragma unroll
    for (int l = 0; l < 7; ++l) { cf v = ldc(rs, ro + (32 * 7 + l) * 16384); z[l] = {z[l].x - v.y, z[l].y + v.x}; }
  }
  const int n1 = 32 * st + r, s = 32 * n1 + n2;
  const float g = __builtin_bit_cast(float, ld32s(rs, WS_GATE + (n2 * 64 + n1) * 4));
  bf16x8 bt[4][2];
#pragma unroll
  for (int ks = 0; ks < 4; ++ks)
#pragma unroll
    for (int hl = 0; hl < 2; ++hl) bt[ks][hl] = *(const bf16x8*)&gT3.v[st][ks][hl][lane][0];
  lds_wait1(lds3, C_ADONE + 1, 4u);
  LAS char* at = lds3 + L_INV;
  {
    cf X0, A[3], B[3], y[7];
    dft7_ab(z, X0, A, B);
    y[0] = X0;
#pragma unroll
    for (int l = 0; l < 3; ++l) {
      y[l + 1] = {A[l].x - B[l].y, A[l].y + B[l].x};
      y[6 - l] = {A[l].x + B[l].y, A[l].y - B[l].x};
    }
#pragma unroll
    for (int m1 = 0; m1 < 7; ++m1) {
      const int rowi = 32 * (m1 >> 2) + 16 * ((m1 >> 1) & 1) + 8 * (e >> 2) + 4 * (m1 & 1) + (e & 3);
      u32 hi = pk_bf16(y[m1].x, y[m1].y);
      u32 lo = pk_bf16(y[m1].x - bflo(hi), y[m1].y - bfhi(hi));
      *(LAS u32*)(at + rowi * 144 + k1 * 4) = hi;
      *(LAS u32*)(at + 64 * 144 + rowi * 144 + k1 * 4) = lo;
    }
  }
  lds_set(lds3, C_INV + w, 1u, lane);
  lds_wait4(lds3, C_INV, 1u);
  f32x16 acc;
#pragma unroll
  for (int i = 0; i < 16; ++i) acc[i] = 0.f;
#pragma unroll
  for (int ks = 0; ks < 4; ++ks) {
    LAS char* ap = at + (32 * t + r) * 144 + ks * 32 + hh * 16;
    bf16x8 ah = *(LAS bf16x8*)ap;
    bf16x8 al = *(LAS bf16x8*)(ap + 64 * 144);
    acc = mfma32(al, bt[ks][0], acc);
    acc = mfma32(ah, bt[ks][1], acc);
    acc = mfma32(ah, bt[ks][0], acc);
  }
  STAMP(12);
  lds_wait4(lds3, C_B, 1u);
  STAMP(13);
  f32x4 bq[4];
#pragma unroll
  for (int qd = 0; qd < 4; ++qd) {
    const LAS f32x4* bp = (const LAS f32x4*)(lds3 + L_BP) + (w * 4 + qd) * 64 + lane;
    bq[qd] = (bp[0] + bp[1024]) + (bp[2048] + bp[3072]);
  }
  const float omg = rbf(1.0f - g);
#pragma unroll
  for (int u = 0; u < 2; ++u) {
    const int gseg = 4 * t + 2 * u + hh;
    if (gseg < 7) {
      u32x4 o;
#pragma unroll
      for (int v = 0; v < 2; ++v)
#pragma unroll
        for (int i2 = 0; i2 < 2; ++i2) {
          float r2[2];
#pragma unroll
          for (int k = 0; k < 2; ++k) {
            const int i = 2 * i2 + k;
            float a = rbf(acc[8 * u + 4 * v + i]);
            float b = rbf(bq[2 * u + v][i]);
            r2[k] = rbf(g * a) + rbf(omg * b);
          }
          o[2 * v + i2] = pk_bf16(r2[0], r2[1]);
        }
      *(u32x4*)(out + (size_t)s * D_ + 64 * gseg + 8 * j) = o;
    }
  }
}

NOPK __global__ __launch_bounds__(512, 2) __attribute__((amdgpu_waves_per_eu(2, 2)))
void mega_kernel(const u16* __restrict__ x, const u16* __restrict__ w_r, const u16* __restrict__ w_i,
                 const u16* __restrict__ wg, const u16* __restrict__ wu, const u16* __restrict__ wd,
                 const u16* __restrict__ gw, char* __restrict__ ws, u16* __restrict__ out) {
  extern __shared__ __attribute__((aligned(16))) char lds[];
  LAS char* lds3 = (LAS char*)lds;
  const int tid = threadIdx.x, w = __builtin_amdgcn_readfirstlane(tid >> 6), lane = tid & 63;
  const int b = blockIdx.x, xcd = b & 7, idx = b >> 3;
  rsrc_t rs = __builtin_amdgcn_make_buffer_rsrc(ws, 0, WS_END, 0x00020000);
  if (tid < 64) *(LAS u32*)(lds3 + L_CTL + tid * 4) = 0u;
  wg_barrier();
  STAMP(0);
  const bool gemm = idx < 28;
  const int rb = (xcd >> 2) * 4 + (idx & 3), fb = (xcd & 3) * 7 + (idx >> 2);
  const int T = 8 * rb + ((w + 4 * (fb & 1)) & 7);
  const int hfo_mine = OFF_HF + ((rb * 2 + ((T >> 2) & 1)) * 32 + fb) * 8;
  const int n2 = xcd * 4 + (idx & 3), j = idx >> 2;
  if (w < 4) {
    const int item = xcd * 29 + idx;
    const bool has_item = idx < 29 && item < 231;
    const int itc = has_item ? item : 0;
    FftPre fp;
    cf v[8];
    f32x4 acc[2][4][2];
    u32x4 pfa, pfb;
    if (gemm) {
      __builtin_amdgcn_s_setprio(2);
      ffn1_kloop<true>(x, wg, wu, T, fb, lds3, w, lane, acc);
      __builtin_amdgcn_s_setprio(0);
      STAMP(4);
    }
    fft_prefetch(fp, w_r, w_i, itc, w, lane);
    pfa = ld128s(rs, OFF_PF + xcd * 2048 + lane * 16);
    pfb = ld128s(rs, OFF_PF + xcd * 2048 + 1024 + lane * 16);
    SB0();
    if (gemm) ffn1_epilogue(rs, T, fb, lane, acc);
    else aux_phase(x, wd, gw, rs, xcd * 4 + (idx - 28), lds3, w, lane);
    STAMP(5);
    const u32x2 tag = get_tag(lds3);
    const bool early = __builtin_amdgcn_ballot_w64(!(tag_ok2(pfa, tag) && tag_ok2(pfb, tag))) == 0ull;
    __builtin_amdgcn_fence(__ATOMIC_ACQUIRE, "wavefront");
    if (early) fft_loadP(v, rs, itc, w, lane);
    DRAIN();
    if (lds_arrive(lds3, C_ADONE, lane) == 3u) {
      if (gemm) { if (lane == 0) st64s(rs, hfo_mine, tag); }
      else if (lane < 8) st64s(rs, OFF_WF + lane * 256 + (xcd * 4 + (idx - 28)) * 8, tag);
    }
    STAMP(6);
    if (has_item) {
      if (!early) { poll256(rs, OFF_PF + xcd * 2048, tag, lane); fft_loadP(v, rs, itc, w, lane); }
      STAMP(7);
      fft_item(rs, fp, v, item, lds3, w, lane);
      STAMP(9);
      DRAIN();
    }
    if (lds_arrive(lds3, C_RCNT, lane) == 3u) {
      if (lane < 8) st64s(rs, OFF_RF + lane * 2048 + b * 8, tag);
    }
    STAMP(10);
    if (w == 0) {
      poll_lanes(rs, OFF_WF + xcd * 256 + (lane & 31) * 8, true, tag, lane);
      poll256(rs, OFF_RF + xcd * 2048, tag, lane);
      lds_set(lds3, C_RGO, 1u, lane);
      if (b == 0 && lane == 0) st64s(rs, OFF_E, tag);
    } else {
      lds_wait1(lds3, C_RGO, 1u);
    }
    STAMP(11);
    final_phase(rs, out, n2, j, lds3, w, lane);
    STAMP(14);
  } else {
    const int bw = w - 4;
    dft_piece(x, lds3, rs, n2, j, bw, lane);
    STAMP(1);
    const u32x2 tag = get_tag(lds3);
    f32x4 acc[2][4][2];
    if (gemm) {
      ffn1_kloop<false>(x, wg, wu, T, fb, lds3, w, lane, acc);
      STAMP(4);
    }
    const int rho = lane & 31, hh = lane >> 5;
    const int hl = (n2 >> 1) & 1;
    const bool pad = (rho & 20) == 20;
    const int wbase = WS_WD + (j * 112 + bw) * 1792;
    const int wo0 = wbase + lane * 16;
    const int wo1 = pad ? (int)0x7ff00000 : wbase + (64 + hh * 24 + (rho < 20 ? rho : rho - 4)) * 16;
    const int ho0 = WS_H + ((2 * n2) * 112 + bw) * 1024 + lane * 16;
    const int e4 = 4 * hl, l4 = 4 - e4;
    u32x4 wdv[14][2], hb[7][2];
#define LDWD(h4, s) do { wdv[s][0] = __builtin_amdgcn_raw_buffer_load_b128(rs, wo0, ((h4) + 8 * (s)) * 1792, AUX_SC1); \
                         wdv[s][1] = __builtin_amdgcn_raw_buffer_load_b128(rs, wo1, ((h4) + 8 * (s)) * 1792, AUX_SC1); } while (0)
#define LDH(h4, s) do { hb[(s) % 7][0] = __builtin_amdgcn_raw_buffer_load_b128(rs, ho0, ((h4) + 8 * (s)) * 1024, AUX_SC1); \
                        hb[(s) % 7][1] = __builtin_amdgcn_raw_buffer_load_b128(rs, ho0, ((h4) + 112 + 8 * (s)) * 1024, AUX_SC1); } while (0)
#define MM(s) do { \
    bc[0][0] = mfma32(as_frag(wdv[s][0]), as_frag(hb[(s) % 7][0]), bc[0][0]); \
    bc[0][1] = mfma32(as_frag(wdv[s][0]), as_frag(hb[(s) % 7][1]), bc[0][1]); \
    bc[1][0] = mfma32(as_frag(wdv[s][1]), as_frag(hb[(s) % 7][0]), bc[1][0]); \
    bc[1][1] = mfma32(as_frag(wdv[s][1]), as_frag(hb[(s) % 7][1]), bc[1][1]); } while (0)
    poll_lanes(rs, OFF_WF + xcd * 256 + (lane & 31) * 8, true, tag, lane);
    STAMP(7);
#pragma unroll
    for (int s = 0; s < 14; ++s) LDWD(e4, s);
    const int hfo = OFF_HF + (((n2 >> 2) * 2 + hl) * 32 + 2 * (lane < 14 ? lane : 0)) * 8;
    const int hfe = hfo + hl * 8, hfl = hfo + (1 - hl) * 8;
    u32x2 fe = ld64s(rs, hfe);
    SB0();
    if (gemm) ffn1_epilogue(rs, T, fb, lane, acc);
    STAMP(5);
    const bool early = flag_ok(fe, tag, true);
    __builtin_amdgcn_fence(__ATOMIC_ACQUIRE, "wavefront");
    if (early) {
#pragma unroll
      for (int s = 0; s < 7; ++s) LDH(e4, s);
    }
    DRAIN();
    if (lds_arrive(lds3, C_ADONE + 1, lane) == 3u) {
      if (gemm && lane == 0) st64s(rs, hfo_mine, tag);
    }
    STAMP(6);
    if (!early) {
      poll_lanes(rs, hfe, true, tag, lane);
#pragma unroll
      for (int s = 0; s < 7; ++s) LDH(e4, s);
    }
    STAMP(8);
    f32x16 bc[2][2];
#pragma unroll
    for (int a = 0; a < 2; ++a)
#pragma unroll
      for (int c = 0; c < 2; ++c)
#pragma unroll
        for (int i = 0; i < 16; ++i) bc[a][c][i] = 0.f;
    SB0();
#pragma unroll
    for (int s = 0; s < 7; ++s) { MM(s); SB0(); LDH(e4, s + 7); LDWD(l4, s); SB0(); }
    u32x2 fb2 = ld64s(rs, hfl);
    SB0();
#pragma unroll
    for (int s = 7; s < 14; ++s) { MM(s); SB0(); LDWD(l4, s); SB0(); }
    STAMP(9);
    if (!flag_ok(fb2, tag, true)) poll_lanes(rs, hfl, true, tag, lane);
    __builtin_amdgcn_fence(__ATOMIC_ACQUIRE, "wavefront");
#pragma unroll
    for (int s = 0; s < 7; ++s) LDH(l4, s);
    SB0();
#pragma unroll
    for (int s = 0; s < 7; ++s) { MM(s); SB0(); LDH(l4, s + 7); SB0(); }
#pragma unroll
    for (int s = 7; s < 14; ++s) { MM(s); SB0(); }
#undef LDWD
#undef LDH
#undef MM
    STAMP(10);
    lds_wait1(lds3, C_ADONE, 4u);
    lds_wait1(lds3, C_ADONE + 1, 4u);
    LAS f32x4* bp = (LAS f32x4*)(lds3 + L_BP + bw * 16384) + lane;
#pragma unroll
    for (int t = 0; t < 2; ++t)
#pragma unroll
      for (int st = 0; st < 2; ++st)
#pragma unroll
        for (int qd = 0; qd < 4; ++qd)
          bp[((t * 2 + st) * 4 + qd) * 64] = f32x4{bc[t][st][4 * qd], bc[t][st][4 * qd + 1], bc[t][st][4 * qd + 2], bc[t][st][4 * qd + 3]};
    lds_set(lds3, C_B + bw, 1u, lane);
    STAMP(11);
  }
  STAMP_DUMP();
}

extern "C" void kernel_launch(void* const* d_in, const int* in_sizes, int n_in, void* d_out, int out_size,
                              void* d_ws, size_t ws_size, hipStream_t stream) {
  (void)hipFuncSetAttribute((const void*)mega_kernel, hipFuncAttributeMaxDynamicSharedMemorySize, LDS_BYTES);
  mega_kernel<<<256, 512, LDS_BYTES, stream>>>((const u16*)d_in[0], (const u16*)d_in[1], (const u16*)d_in[2],
                                               (const u16*)d_in[4], (const u16*)d_in[5], (const u16*)d_in[6],
                                               (const u16*)d_in[7], (char*)d_ws, (u16*)d_out);
}
```

```cpp
#include <hip/hip_runtime.h>
#include <hip/hip_bf16.h>

typedef unsigned short u16;
typedef unsigned int u32;
typedef __attribute__((ext_vector_type(8))) short bf16x8;
typedef __attribute__((ext_vector_type(4))) short s16x4;
typedef __attribute__((ext_vector_type(4))) float f32x4;
typedef __attribute__((ext_vector_type(2))) float f32x2;
typedef __attribute__((ext_vector_type(16))) float f32x16;
typedef __attribute__((ext_vector_type(4))) unsigned int u32x4;
typedef __attribute__((ext_vector_type(2))) unsigned int u32x2;
typedef __attribute__((ext_vector_type(2))) __bf16 bf16x2;

#if defined(__HIP_DEVICE_COMPILE__)
#define NOPK __attribute__((target("no-packed-fp32-ops")))
#else
#define NOPK
#endif
#define DEV NOPK __device__ __forceinline__

constexpr int S_ = 2048, D_ = 448, F_ = 1792, DF_ = 225;

constexpr double kPi = 3.14159265358979323846264338327950288;
constexpr double tsin(double x) {
  double t = x, s = x;
  for (int i = 1; i < 12; ++i) { t *= -x * x / ((2 * i) * (2 * i + 1)); s += t; }
  return s;
}
constexpr double tcos(double x) {
  double t = 1, s = 1;
  for (int i = 1; i < 12; ++i) { t *= -x * x / ((2 * i - 1) * (2 * i)); s += t; }
  return s;
}
constexpr double cosn(long n, long N) {
  n %= N; if (n < 0) n += N;
  if (2 * n > N) n = N - n;
  if (4 * n > N) return -cosn(N - 2 * n, 2 * N) ;
  if (8 * n > N) return tsin(2 * kPi * (double)(N - 4 * n) / (double)(4 * N));
  return tcos(2 * kPi * (double)n / (double)N);
}
constexpr double sinn(long n, long N) { return cosn(4 * n - N, 4 * N); }

constexpr u16 f2bf(float f) {
  u32 u = __builtin_bit_cast(u32, f);
  return (u16)((u + 0x7FFFu + ((u >> 16) & 1u)) >> 16);
}
constexpr float bf2f(u16 h) { return __builtin_bit_cast(float, (u32)h << 16); }

struct C64 { double c[64], s[64]; };
constexpr C64 mkc64() { C64 t{}; for (int i = 0; i < 64; ++i) { t.c[i] = cosn(i, 64); t.s[i] = sinn(i, 64); } return t; }
constexpr C64 c64 = mkc64();

struct FragTab { u16 v[2][4][2][64][8]; };
constexpr FragTab mkT1() {
  FragTab t{};
  for (int mt = 0; mt < 2; ++mt) for (int ks = 0; ks < 4; ++ks) for (int l = 0; l < 64; ++l) for (int j = 0; j < 8; ++j) {
    int rho = l & 31, hh = (rho >> 2) & 1, reg = (rho & 3) + 4 * (rho >> 3);
    int k1 = 16 * mt + (reg >> 1) + 8 * hh, part = reg & 1;
    int n1 = 16 * ks + 8 * (l >> 5) + j;
    double val = part ? -c64.s[(n1 * k1) & 63] : c64.c[(n1 * k1) & 63];
    if (k1 == 0 && part) val = (n1 & 1) ? -1.0 : 1.0;
    u16 hi = f2bf((float)val);
    u16 lo = f2bf((float)(val - (double)bf2f(hi)));
    t.v[mt][ks][0][l][j] = hi; t.v[mt][ks][1][l][j] = lo;
  }
  return t;
}
constexpr FragTab mkT3() {
  FragTab t{};
  for (int st = 0; st < 2; ++st) for (int ks = 0; ks < 4; ++ks) for (int l = 0; l < 64; ++l) for (int j = 0; j < 8; ++j) {
    int n1 = 32 * st + (l & 31), kap = 16 * ks + 8 * (l >> 5) + j;
    int k1 = kap >> 1, part = kap & 1;
    double val = part ? -2.0 * c64.s[(n1 * k1) & 63] : 2.0 * c64.c[(n1 * k1) & 63];
    if (k1 == 0) val = part ? ((n1 & 1) ? -1.0 : 1.0) : 1.0;
    val /= (double)(S_ * D_);
    u16 hi = f2bf((float)val);
    u16 lo = f2bf((float)(val - (double)bf2f(hi)));
    t.v[st][ks][0][l][j] = hi; t.v[st][ks][1][l][j] = lo;
  }
  return t;
}
struct Tw2048 { float v[2048][2]; };
struct Tw448 { float v[448][2]; };
constexpr Tw2048 mkTw2048() { Tw2048 t{}; for (int i = 0; i < 2048; ++i) { t.v[i][0] = (float)cosn(i, 2048); t.v[i][1] = (float)-sinn(i, 2048); } return t; }
constexpr Tw448 mkTw448() { Tw448 t{}; for (int i = 0; i < 448; ++i) { t.v[i][0] = (float)cosn(i, 448); t.v[i][1] = (float)-sinn(i, 448); } return t; }

static __constant__ __attribute__((aligned(16))) FragTab gT1 = mkT1();
static __constant__ __attribute__((aligned(16))) FragTab gT3 = mkT3();
static __constant__ __attribute__((aligned(16))) Tw2048 gTw2048 = mkTw2048();
static __constant__ __attribute__((aligned(16))) Tw448 gTw448 = mkTw448();

constexpr float C7_1 = (float)cosn(1, 7), C7_2 = (float)cosn(2, 7), C7_3 = (float)cosn(3, 7);
constexpr float S7_1 = (float)sinn(1, 7), S7_2 = (float)sinn(2, 7), S7_3 = (float)sinn(3, 7);

constexpr size_t WS_H = 0;
constexpr size_t WS_WD = WS_H + (size_t)S_ * F_ * 2;
constexpr size_t WS_P = WS_WD + (size_t)F_ * D_ * 2;
constexpr size_t WS_R = WS_P + (size_t)231 * 2048 * 8;
constexpr size_t WS_PART = WS_R + (size_t)231 * 2048 * 8;
constexpr size_t WS_GATE = WS_PART + (size_t)4 * 32 * 8 * 16 * 1024;

DEV u32 pk_bf16(float a, float b) { bf16x2 v = {(__bf16)a, (__bf16)b}; return __builtin_bit_cast(u32, v); }
DEV float bflo(u32 v) { return __builtin_bit_cast(float, v << 16); }
DEV float bfhi(u32 v) { return __builtin_bit_cast(float, v & 0xffff0000u); }
DEV float rbf(float a) { return (float)(__bf16)a; }
DEV u32 perm_lo(u32 hi_src, u32 lo_src) { return __builtin_amdgcn_perm(hi_src, lo_src, 0x05040100u); }
DEV u32 perm_hi(u32 hi_src, u32 lo_src) { return __builtin_amdgcn_perm(hi_src, lo_src, 0x07060302u); }
DEV bf16x8 as_frag(u32x4 v) { return __builtin_bit_cast(bf16x8, v); }
DEV f32x4 mfma16(bf16x8 a, bf16x8 b, f32x4 c) { return __builtin_amdgcn_mfma_f32_16x16x32_bf16(a, b, c, 0, 0, 0); }
DEV f32x16 mfma32(bf16x8 a, bf16x8 b, f32x16 c) { return __builtin_amdgcn_mfma_f32_32x32x16_bf16(a, b, c, 0, 0, 0); }

DEV void wg_barrier() {
  __builtin_amdgcn_fence(__ATOMIC_RELEASE, "workgroup");
  __builtin_amdgcn_s_barrier();
  __builtin_amdgcn_fence(__ATOMIC_ACQUIRE, "workgroup");
}
#define STAMP(slot) do {} while (0)

struct cf { float x, y; };
DEV cf cadd(cf a, cf b) { return {a.x + b.x, a.y + b.y}; }
DEV cf csub(cf a, cf b) { return {a.x - b.x, a.y - b.y}; }
DEV cf cmul(cf a, cf b) { return {a.x * b.x - a.y * b.y, a.x * b.y + a.y * b.x}; }
DEV cf cmulc(cf a, cf b) { return {a.x * b.x + a.y * b.y, a.y * b.x - a.x * b.y}; }
template <int INV> DEV cf ctw(cf a, cf w) { return INV ? cmulc(a, w) : cmul(a, w); }
template <int INV> DEV cf rot90(cf a) { return INV ? cf{-a.y, a.x} : cf{a.y, -a.x}; }

template <int INV> DEV void dft4(cf& a0, cf& a1, cf& a2, cf& a3) {
  cf t0 = cadd(a0, a2), t1 = csub(a0, a2), t2 = cadd(a1, a3), t3 = rot90<INV>(csub(a1, a3));
  a0 = cadd(t0, t2); a2 = csub(t0, t2); a1 = cadd(t1, t3); a3 = csub(t1, t3);
}
template <int INV> DEV void dft8(cf (&v)[8]) {
  constexpr float R = 0.70710678118654752440f;
  cf e0 = v[0], e1 = v[2], e2 = v[4], e3 = v[6], o0 = v[1], o1 = v[3], o2 = v[5], o3 = v[7];
  dft4<INV>(e0, e1, e2, e3);
  dft4<INV>(o0, o1, o2, o3);
  cf p1 = INV ? cf{(o1.x - o1.y) * R, (o1.x + o1.y) * R} : cf{(o1.x + o1.y) * R, (o1.y - o1.x) * R};
  cf p2 = rot90<INV>(o2);
  cf p3 = INV ? cf{(-o3.x - o3.y) * R, (o3.x - o3.y) * R} : cf{(o3.y - o3.x) * R, (-o3.x - o3.y) * R};
  v[0] = cadd(e0, o0); v[4] = csub(e0, o0);
  v[1] = cadd(e1, p1); v[5] = csub(e1, p1);
  v[2] = cadd(e2, p2); v[6] = csub(e2, p2);
  v[3] = cadd(e3, p3); v[7] = csub(e3, p3);
}
DEV void dft7_ab(const cf (&z)[7], cf& X0, cf (&A)[3], cf (&B)[3]) {
  cf t1 = cadd(z[1], z[6]), t2 = cadd(z[2], z[5]), t3 = cadd(z[3], z[4]);
  cf s1 = csub(z[1], z[6]), s2 = csub(z[2], z[5]), s3 = csub(z[3], z[4]);
  X0 = {z[0].x + t1.x + t2.x + t3.x, z[0].y + t1.y + t2.y + t3.y};
  A[0] = {z[0].x + C7_1 * t1.x + C7_2 * t2.x + C7_3 * t3.x, z[0].y + C7_1 * t1.y + C7_2 * t2.y + C7_3 * t3.y};
  A[1] = {z[0].x + C7_2 * t1.x + C7_3 * t2.x + C7_1 * t3.x, z[0].y + C7_2 * t1.y + C7_3 * t2.y + C7_1 * t3.y};
  A[2] = {z[0].x + C7_3 * t1.x + C7_1 * t2.x + C7_2 * t3.x, z[0].y + C7_3 * t1.y + C7_1 * t2.y + C7_2 * t3.y};
  B[0] = {S7_1 * s1.x + S7_2 * s2.x + S7_3 * s3.x, S7_1 * s1.y + S7_2 * s2.y + S7_3 * s3.y};
  B[1] = {S7_2 * s1.x - S7_3 * s2.x - S7_1 * s3.x, S7_2 * s1.y - S7_3 * s2.y - S7_1 * s3.y};
  B[2] = {S7_3 * s1.x - S7_1 * s2.x + S7_2 * s3.x, S7_3 * s1.y - S7_1 * s2.y + S7_2 * s3.y};
}

DEV float gelu_mul(float g, float u) {
  const float c1 = -2.0f * 1.4426950408889634f * 0.7978845608028654f;
  const float c2 = c1 * 0.044715f;
  float a = g * __builtin_fmaf(g * g, c2, c1);
  float e = __builtin_amdgcn_exp2f(a);
  float r = __builtin_amdgcn_rcpf(1.0f + e);
  return (g * u) * r;
}

constexpr int XS = 928;
constexpr int K1_XCH = 128 * XS;
constexpr int K1_LDS = K1_XCH + 8 * 4096;
constexpr int AXS = 960;
constexpr int PF = 3;

DEV void k1_gemm(const u16* __restrict__ x, const u16* __restrict__ wg, const u16* __restrict__ wu,
                 u32x4* __restrict__ h_t, int sb, int ft, char* lds) {
  const int tid = threadIdx.x, w = __builtin_amdgcn_readfirstlane(tid >> 6), lane = tid & 63, r = lane & 15, q = lane >> 4;
  const int mat = w >> 2, f0 = 128 * ft + 32 * (w & 3);
  const u16* wp = (mat ? wu : wg) + (size_t)(8 * q) * F_ + f0 + 2 * r;
  const int row0 = tid >> 3, piece = tid & 7;
  const u16* xp0 = x + (size_t)(32 * row0 + 2 * sb) * D_ + piece * 8;
  const u16* xp1 = xp0 + D_;
  char* xw0 = lds + row0 * XS + piece * 16;
  char* xw1 = xw0 + 64 * XS;
  const char* xr = lds + r * XS + q * 16;

  u32x4 xs[7][2];
  u32 wr[14][8];
  f32x4 acc[2][8];
#pragma unroll
  for (int a = 0; a < 2; ++a)
#pragma unroll
    for (int b = 0; b < 8; ++b) acc[a][b] = f32x4{0.f, 0.f, 0.f, 0.f};

#define K1_LOAD(c)                                                                       \
  do {                                                                                   \
    xs[c][0] = *(const u32x4*)(xp0 + (c) * 64);                                          \
    xs[c][1] = *(const u32x4*)(xp1 + (c) * 64);                                          \
    _Pragma("unroll") for (int kk = 0; kk < 2; ++kk)                                     \
    _Pragma("unroll") for (int j = 0; j < 8; ++j)                                        \
        wr[2 * (c) + kk][j] = *(const u32*)(wp + (size_t)(64 * (c) + 32 * kk + j) * F_); \
  } while (0)

  STAMP(0);
#pragma unroll
  for (int c = 0; c < PF; ++c) K1_LOAD(c);
  __builtin_amdgcn_sched_barrier(0);
  *(u32x4*)(xw0) = xs[0][0];
  *(u32x4*)(xw1) = xs[0][1];
#pragma unroll
  for (int c = 0; c < 7; ++c) {
    wg_barrier();
    STAMP(1 + c);
    if (c + PF < 7) K1_LOAD(c + PF);
    if (c + 1 < 7) {
      *(u32x4*)(xw0 + (c + 1) * 128) = xs[c + 1][0];
      *(u32x4*)(xw1 + (c + 1) * 128) = xs[c + 1][1];
    }
    __builtin_amdgcn_sched_barrier(0);
#pragma unroll
    for (int kk = 0; kk < 2; ++kk) {
      const int ks = 2 * c + kk;
      u32x4 a0, a1;
#pragma unroll
      for (int p = 0; p < 4; ++p) {
        a0[p] = perm_lo(wr[ks][2 * p + 1], wr[ks][2 * p]);
        a1[p] = perm_hi(wr[ks][2 * p + 1], wr[ks][2 * p]);
      }
#pragma unroll
      for (int st = 0; st < 8; ++st) {
        bf16x8 b = *(const bf16x8*)(xr + st * 16 * XS + ks * 64);
        acc[0][st] = mfma16(as_frag(a0), b, acc[0][st]);
        acc[1][st] = mfma16(as_frag(a1), b, acc[1][st]);
      }
    }
  }
#undef K1_LOAD
  STAMP(8);
  {
    u32x4* xo = (u32x4*)(lds + K1_XCH + w * 4096) + lane;
    const int so = mat ? 0 : 4;
#pragma unroll
    for (int i = 0; i < 4; ++i) {
      f32x4 v0 = mat ? acc[0][i] : acc[0][4 + i];
      f32x4 v1 = mat ? acc[1][i] : acc[1][4 + i];
      u32x4 o;
#pragma unroll
      for (int g = 0; g < 4; ++g) o[g] = pk_bf16(v0[g], v1[g]);
      xo[i * 64] = o;
    }
    (void)so;
    wg_barrier();
    const u32x4* xi = (const u32x4*)(lds + K1_XCH + (w ^ 4) * 4096) + lane;
    const int ksf = f0 >> 5;
#pragma unroll
    for (int i = 0; i < 4; ++i) {
      u32x4 o = xi[i * 64];
      f32x4 v0 = mat ? acc[0][4 + i] : acc[0][i];
      f32x4 v1 = mat ? acc[1][4 + i] : acc[1][i];
      u32x4 hv;
#pragma unroll
      for (int g = 0; g < 4; ++g) {
        float p0 = bflo(o[g]), p1 = bfhi(o[g]);
        float g0 = mat ? p0 : v0[g], u0 = mat ? v0[g] : p0;
        float g1 = mat ? p1 : v1[g], u1 = mat ? v1[g] : p1;
        hv[g] = pk_bf16(gelu_mul(g0, u0), gelu_mul(g1, u1));
      }
      const int sig = 8 * sb + (mat ? 4 : 0) + i;
      h_t[((size_t)sig * 56 + ksf) * 64 + lane] = hv;
    }
  }
  STAMP(9);
}

DEV void k1_aux(const u16* __restrict__ x, const u16* __restrict__ wd, const u16* __restrict__ gw,
                f32x2* __restrict__ P, u32x4* __restrict__ wd_t, float* __restrict__ gate, int n2, char* lds) {
  const int tid = threadIdx.x, w = __builtin_amdgcn_readfirstlane(tid >> 6), lane = tid & 63;
  STAMP(0);
  {
    u32x4 t[7];
#pragma unroll
    for (int i = 0; i < 7; ++i) {
      int id = tid + 512 * i, row = id / 56, pc = id % 56;
      t[i] = *(const u32x4*)(x + (size_t)(32 * row + n2) * D_ + pc * 8);
    }
#pragma unroll
    for (int i = 0; i < 7; ++i) {
      int id = tid + 512 * i, row = id / 56, pc = id % 56;
      *(u32x4*)(lds + row * AXS + pc * 16) = t[i];
    }
  }
  if (w < 4) {
    const int mt = w >> 1, mh = w & 1, hh = lane >> 5;
    bf16x8 af[4][2];
#pragma unroll
    for (int ks = 0; ks < 4; ++ks)
#pragma unroll
      for (int hl = 0; hl < 2; ++hl) af[ks][hl] = *(const bf16x8*)&gT1.v[mt][ks][hl][lane][0];
    wg_barrier();
    STAMP(1);
    f32x16 acc[7];
    const int blk = (lane >> 4) & 1, qq = (lane & 15) >> 2, pp = lane & 3;
    const char* tb = lds + (8 * hh + qq) * AXS + (32 * mh + 16 * blk + 4 * pp) * 2;
#pragma unroll
    for (int m1 = 0; m1 < 7; ++m1) {
#pragma unroll
      for (int i = 0; i < 16; ++i) acc[m1][i] = 0.f;
#pragma unroll
      for (int ks = 0; ks < 4; ++ks) {
        const char* p = tb + 16 * ks * AXS + m1 * 128;
        s16x4 lo = __builtin_amdgcn_ds_read_tr16_b64_v4i16((__attribute__((address_space(3))) s16x4*)(p));
        s16x4 hi = __builtin_amdgcn_ds_read_tr16_b64_v4i16((__attribute__((address_space(3))) s16x4*)(p + 4 * AXS));
        bf16x8 b = __builtin_shufflevector(lo, hi, 0, 1, 2, 3, 4, 5, 6, 7);
        acc[m1] = mfma32(af[ks][0], b, acc[m1]);
        acc[m1] = mfma32(af[ks][1], b, acc[m1]);
      }
    }
    const int m2 = 32 * mh + (lane & 31);
    STAMP(2);
#pragma unroll
    for (int i = 0; i < 8; ++i) {
      cf z[7];
#pragma unroll
      for (int m1 = 0; m1 < 7; ++m1) z[m1] = {acc[m1][2 * i], acc[m1][2 * i + 1]};
      cf X0, A[3], B[3];
      dft7_ab(z, X0, A, B);
      const int k1 = 16 * mt + i + 8 * hh;
      f32x2* pp0 = P + ((size_t)(k1 * 7) * 32 + n2) * 64 + m2;
      if (i == 0 && mt == 0 && hh == 0) {
        f32x2* pp32 = P + ((size_t)(32 * 7) * 32 + n2) * 64 + m2;
        pp0[0] = f32x2{X0.x, 0.f};
        pp32[0] = f32x2{X0.y, 0.f};
#pragma unroll
        for (int l = 0; l < 3; ++l) {
          pp0[(size_t)(l + 1) * 2048] = f32x2{A[l].x, -B[l].x};
          pp0[(size_t)(6 - l) * 2048] = f32x2{A[l].x, B[l].x};
          pp32[(size_t)(l + 1) * 2048] = f32x2{A[l].y, -B[l].y};
          pp32[(size_t)(6 - l) * 2048] = f32x2{A[l].y, B[l].y};
        }
      } else {
        pp0[0] = f32x2{X0.x, X0.y};
#pragma unroll
        for (int l = 0; l < 3; ++l) {
          pp0[(size_t)(l + 1) * 2048] = f32x2{A[l].x + B[l].y, A[l].y - B[l].x};
          pp0[(size_t)(6 - l) * 2048] = f32x2{A[l].x - B[l].y, A[l].y + B[l].x};
        }
      }
    }
    STAMP(9);
  } else {
    const int t2 = tid - 256;
#pragma unroll
    for (int it = 0; it < 2; ++it) {
      int id = t2 + 256 * it;
      if (id < 392) {
        int grp = id / 56, seg = id % 56;
        int g = 7 * n2 + grp;
        const u16* src = wd + (size_t)(8 * g) * D_ + seg * 8;
        u32x4 rw[8];
#pragma unroll
        for (int j = 0; j < 8; ++j) rw[j] = *(const u32x4*)(src + (size_t)j * D_);
        int d0 = seg * 8, dq = d0 / 112, rem = d0 % 112, tau = rem >> 4, r0 = rem & 15;
        u32x4* dst = wd_t + ((size_t)(dq * 56 + (g >> 2)) * 7 + tau) * 64 + (g & 3) * 16 + r0;
#pragma unroll
        for (int e2 = 0; e2 < 4; ++e2) {
          u32x4 o0, o1;
#pragma unroll
          for (int p = 0; p < 4; ++p) {
            o0[p] = perm_lo(rw[2 * p + 1][e2], rw[2 * p][e2]);
            o1[p] = perm_hi(rw[2 * p + 1][e2], rw[2 * p][e2]);
          }
          dst[2 * e2] = o0;
          dst[2 * e2 + 1] = o1;
        }
      }
    }
    float gwf[8];
    {
      u32x4 gv = (lane < 56) ? *(const u32x4*)(gw + lane * 8) : u32x4{0u, 0u, 0u, 0u};
#pragma unroll
      for (int p = 0; p < 4; ++p) { gwf[2 * p] = bflo(gv[p]); gwf[2 * p + 1] = bfhi(gv[p]); }
    }
    wg_barrier();
    STAMP(1);
    const int lc = lane < 56 ? lane : 0;
    float mine = 0.f;
#pragma unroll 4
    for (int i = 0; i < 16; ++i) {
      const int n1 = 16 * (w - 4) + i;
      u32x4 xv = *(const u32x4*)(lds + n1 * AXS + lc * 16);
      float sacc = 0.f;
#pragma unroll
      for (int p = 0; p < 4; ++p) {
        u32 pr = pk_bf16(bflo(xv[p]) * gwf[2 * p], bfhi(xv[p]) * gwf[2 * p + 1]);
        sacc += bflo(pr);
        sacc += bfhi(pr);
      }
#pragma unroll
      for (int o = 32; o >= 1; o >>= 1)
        sacc += __builtin_bit_cast(float, __builtin_amdgcn_ds_bpermute((lane ^ o) << 2, __builtin_bit_cast(int, sacc)));
      if (lane == i) mine = sacc;
    }
    if (lane < 16) {
      float z = rbf(mine);
      float g = __builtin_amdgcn_rcpf(1.0f + __builtin_amdgcn_exp2f(-1.4426950408889634f * z));
      gate[32 * (16 * (w - 4) + lane) + n2] = rbf(g);
    }
    STAMP(9);
  }
}

NOPK __global__ __launch_bounds__(512, 2) void k1_kernel(const u16* __restrict__ x, const u16* __restrict__ wg,
                                                    const u16* __restrict__ wu, const u16* __restrict__ wd,
                                                    const u16* __restrict__ gw, char* __restrict__ ws) {
  extern __shared__ __attribute__((aligned(16))) char lds[];
  const int b = blockIdx.x, xcd = b & 7, idx = b >> 3;
  if (idx < 28) {
    const int id = xcd * 28 + idx;
    const int sh = id / 112, rem = id % 112, ft = rem >> 3, sb = sh * 8 + (rem & 7);
    k1_gemm(x, wg, wu, (u32x4*)(ws + WS_H), sb, ft, lds);
  } else {
    k1_aux(x, wd, gw, (f32x2*)(ws + WS_P), (u32x4*)(ws + WS_WD), (float*)(ws + WS_GATE), xcd * 4 + (idx - 28), lds);
  }
}

constexpr int K2_WD = 0;
constexpr int K2_FFT = 98 * 1024;
constexpr int K2_FLAG = K2_FFT + 4 * 4608;
constexpr int K2_LDS = K2_FLAG + 64;

typedef __attribute__((address_space(3))) volatile u32 lds_vu32;
typedef __attribute__((address_space(3))) volatile u32x4 lds_vu32x4;
DEV void sub_arrive_wait(volatile u32* flags4g, int mywave, u32 phase, int lane) {
  lds_vu32* flags4 = (lds_vu32*)flags4g;
  __builtin_amdgcn_fence(__ATOMIC_RELEASE, "workgroup");
  if (lane == 0) flags4[mywave] = phase;
  for (int spin = 0; spin < (1 << 22); ++spin) {
    u32x4 f = *(lds_vu32x4*)flags4;
    u32 m0 = f[0] < f[1] ? f[0] : f[1], m1 = f[2] < f[3] ? f[2] : f[3], m = m0 < m1 ? m0 : m1;
    if (__builtin_amdgcn_readfirstlane(m) >= phase) break;
    __builtin_amdgcn_s_sleep(1);
  }
  __builtin_amdgcn_fence(__ATOMIC_ACQUIRE, "workgroup");
}

DEV void k2_gemm(const u32x4* __restrict__ h_t, const u32x4* __restrict__ wd_t, f32x4* __restrict__ part,
                 int b, int dq, int q, char* lds) {
  const int tid = threadIdx.x, w = __builtin_amdgcn_readfirstlane(tid >> 6), lane = tid & 63;
  const int T = 4 * b + w;
  const u32x4* wsrc = wd_t + (size_t)(dq * 56 + 14 * q) * 7 * 64 + lane;
  u32x4* wl = (u32x4*)(lds + K2_WD) + lane;
  const u32x4* hsrc0 = h_t + ((size_t)(2 * T) * 56 + 14 * q) * 64 + lane;
  const u32x4* hsrc1 = hsrc0 + (size_t)56 * 64;
  STAMP(0);
  {
    u32x4 t[25];
#pragma unroll
    for (int n = 0; n < 25; ++n) { int i = w + 4 * n; if (n < 24 || w < 2) t[n] = wsrc[i * 64]; }
#pragma unroll
    for (int n = 0; n < 25; ++n) { int i = w + 4 * n; if (n < 24 || w < 2) wl[i * 64] = t[n]; }
  }
  u32x4 hb[14][2];
#pragma unroll
  for (int i = 0; i < 14; ++i) { hb[i][0] = hsrc0[i * 64]; hb[i][1] = hsrc1[i * 64]; }
  STAMP(1);
  sub_arrive_wait((volatile u32*)(lds + K2_FLAG), w, 1u, lane);
  STAMP(2);
  f32x4 acc[7][2];
#pragma unroll
  for (int t = 0; t < 7; ++t) { acc[t][0] = f32x4{0.f, 0.f, 0.f, 0.f}; acc[t][1] = f32x4{0.f, 0.f, 0.f, 0.f}; }
#pragma unroll
  for (int i = 0; i < 14; ++i) {
#pragma unroll
    for (int t = 0; t < 7; ++t) {
      bf16x8 a = as_frag(wl[(i * 7 + t) * 64]);
      acc[t][0] = mfma16(a, as_frag(hb[i][0]), acc[t][0]);
      acc[t][1] = mfma16(a, as_frag(hb[i][1]), acc[t][1]);
    }
  }
  STAMP(3);
  const int n2 = T >> 1, st = T & 1, c = lane & 15, qq = lane >> 4;
#pragma unroll
  for (int t = 0; t < 7; ++t) {
    const int d = 112 * dq + 16 * t + 4 * qq;
    const int m1 = d >> 6, j = (d >> 3) & 7, v = (d >> 2) & 1;
    const int tt = m1 >> 2, u = (m1 >> 1) & 1, hh = m1 & 1;
    size_t idx = ((((((size_t)(q * 32 + n2) * 8 + j) * 2 + tt) * 2 + st) * 2 + u) * 2 + v) * 64 + hh * 32 + c;
    part[idx] = acc[t][0];
    part[idx + 16] = acc[t][1];
  }
  STAMP(9);
}

DEV cf ldtw2048(int i) { return cf{gTw2048.v[i][0], gTw2048.v[i][1]}; }
DEV cf ldtw448(int i) { return cf{gTw448.v[i][0], gTw448.v[i][1]}; }

DEV void k2_fft(const f32x2* __restrict__ P, f32x2* __restrict__ R, const u16* __restrict__ w_r,
                const u16* __restrict__ w_i, int item, char* lds) {
  const int ft = threadIdx.x - 256, lane = ft & 63;
  const int w = __builtin_amdgcn_readfirstlane(ft >> 6);
  const int k1 = item / 7, l1 = item % 7;
  volatile u32* flags = (volatile u32*)(lds + K2_FLAG + 16);
  char* base = lds + K2_FFT;
  char* mine = base + w * 4608;
  cf v[8];
  STAMP(0);
  {
    const f32x2* src = P + ((size_t)item * 32 + w) * 64 + lane;
#pragma unroll
    for (int c = 0; c < 8; ++c) { f32x2 t = src[c * 256]; v[c] = {t[0], t[1]}; }
  }
  const int row = lane >> 3, lo3 = lane & 7;
  cf wf[8];
  {
    const int k2 = 2 * w + (row >> 2) + 8 * (row & 3);
    const int ks = k1 + 64 * k2, nks = (S_ - ks) & (S_ - 1);
#pragma unroll
    for (int be = 0; be < 8; ++be) {
      const int kd = l1 + 7 * lo3 + 56 * be;
      const bool direct = kd <= 224;
      const int idx = direct ? ks * DF_ + kd : nks * DF_ + (D_ - kd);
      float re = __builtin_bit_cast(float, (u32)w_r[idx] << 16);
      float im = __builtin_bit_cast(float, (u32)w_i[idx] << 16);
      wf[be] = {re, direct ? im : -im};
      if ((be == 0 || be == 4) && (kd == 0 || kd == 224)) {
        const int idx2 = nks * DF_ + kd;
        float re2 = __builtin_bit_cast(float, (u32)w_r[idx2] << 16);
        float im2 = __builtin_bit_cast(float, (u32)w_i[idx2] << 16);
        wf[be] = {0.5f * (re + re2), 0.5f * (im - im2)};
      }
    }
  }
  cf twb[8];
#pragma unroll
  for (int al = 0; al < 8; ++al) twb[al] = ldtw448(lo3 * (7 * al + l1));

  STAMP(1);
#pragma unroll
  for (int c = 1; c < 8; ++c) v[c] = cmul(v[c], ldtw2048(4 * c * k1));
  dft8<0>(v);
#pragma unroll
  for (int g = 0; g < 8; ++g) v[g] = cmul(v[g], ldtw2048(w * (k1 + 64 * g)));
#pragma unroll
  for (int g = 0; g < 8; ++g)
    *(f32x2*)(base + (g >> 1) * 4608 + (((g & 1) * 4 + w) * 64 + lane) * 8) = f32x2{v[g].x, v[g].y};
  STAMP(2);
  sub_arrive_wait(flags, w, 1u, lane);
  STAMP(3);
#pragma unroll
  for (int i = 0; i < 8; ++i) { f32x2 t = *(const f32x2*)(mine + (i * 64 + lane) * 8); v[i] = {t[0], t[1]}; }
  dft4<0>(v[0], v[1], v[2], v[3]);
  dft4<0>(v[4], v[5], v[6], v[7]);
  __builtin_amdgcn_fence(__ATOMIC_ACQ_REL, "wavefront");
#pragma unroll
  for (int i = 0; i < 8; ++i) *(f32x2*)(mine + i * 576 + lane * 8) = f32x2{v[i].x, v[i].y};
  __builtin_amdgcn_fence(__ATOMIC_ACQ_REL, "wavefront");
#pragma unroll
  for (int a = 0; a < 8; ++a) { f32x2 t = *(const f32x2*)(mine + row * 576 + (8 * a + lo3) * 8); v[a] = {t[0], t[1]}; }
#pragma unroll
  for (int a = 1; a < 8; ++a) v[a] = cmul(v[a], ldtw448(8 * a * l1));
  dft8<0>(v);
#pragma unroll
  for (int al = 0; al < 8; ++al) v[al] = cmul(v[al], twb[al]);
  __builtin_amdgcn_fence(__ATOMIC_ACQ_REL, "wavefront");
#pragma unroll
  for (int al = 0; al < 8; ++al) *(f32x2*)(mine + row * 576 + al * 72 + lo3 * 8) = f32x2{v[al].x, v[al].y};
  __builtin_amdgcn_fence(__ATOMIC_ACQ_REL, "wavefront");
#pragma unroll
  for (int bb = 0; bb < 8; ++bb) { f32x2 t = *(const f32x2*)(mine + row * 576 + lo3 * 72 + bb * 8); v[bb] = {t[0], t[1]}; }
  dft8<0>(v);
  STAMP(4);
#pragma unroll
  for (int be = 0; be < 8; ++be) v[be] = cmul(v[be], wf[be]);
  dft8<1>(v);
  __builtin_amdgcn_fence(__ATOMIC_ACQ_REL, "wavefront");
#pragma unroll
  for (int bb = 0; bb < 8; ++bb) *(f32x2*)(mine + row * 576 + lo3 * 72 + bb * 8) = f32x2{v[bb].x, v[bb].y};
  __builtin_amdgcn_fence(__ATOMIC_ACQ_REL, "wavefront");
#pragma unroll
  for (int al = 0; al < 8; ++al) { f32x2 t = *(const f32x2*)(mine + row * 576 + al * 72 + lo3 * 8); v[al] = {t[0], t[1]}; }
#pragma unroll
  for (int al = 0; al < 8; ++al) v[al] = cmulc(v[al], twb[al]);
  dft8<1>(v);
#pragma unroll
  for (int a = 1; a < 8; ++a) v[a] = cmulc(v[a], ldtw448(8 * a * l1));
  __builtin_amdgcn_fence(__ATOMIC_ACQ_REL, "wavefront");
#pragma unroll
  for (int a = 0; a < 8; ++a) *(f32x2*)(mine + row * 576 + (8 * a + lo3) * 8) = f32x2{v[a].x, v[a].y};
  __builtin_amdgcn_fence(__ATOMIC_ACQ_REL, "wavefront");
#pragma unroll
  for (int i = 0; i < 8; ++i) { f32x2 t = *(const f32x2*)(mine + i * 576 + lane * 8); v[i] = {t[0], t[1]}; }
  dft4<1>(v[0], v[1], v[2], v[3]);
  dft4<1>(v[4], v[5], v[6], v[7]);
  __builtin_amdgcn_fence(__ATOMIC_ACQ_REL, "wavefront");
#pragma unroll
  for (int i = 0; i < 8; ++i) *(f32x2*)(mine + (i * 64 + lane) * 8) = f32x2{v[i].x, v[i].y};
  STAMP(5);
  sub_arrive_wait(flags, w, 2u, lane);
  STAMP(6);
#pragma unroll
  for (int g = 0; g < 8; ++g) {
    f32x2 t = *(const f32x2*)(base + (g >> 1) * 4608 + (((g & 1) * 4 + w) * 64 + lane) * 8);
    v[g] = {t[0], t[1]};
  }
#pragma unroll
  for (int g = 0; g < 8; ++g) v[g] = cmulc(v[g], ldtw2048(w * (k1 + 64 * g)));
  dft8<1>(v);
#pragma unroll
  for (int c = 1; c < 8; ++c) v[c] = cmulc(v[c], ldtw2048(4 * c * k1));
#pragma unroll
  for (int c = 0; c < 8; ++c) {
    const int n2 = 4 * c + w;
    R[(((size_t)(n2 * 8 + row) * 7 + l1) * 33 + k1) * 8 + lo3] = f32x2{v[c].x, v[c].y};
  }
  STAMP(9);
}

NOPK __global__ __launch_bounds__(512, 2) void k2_kernel(const u16* __restrict__ w_r, const u16* __restrict__ w_i,
                                                    char* __restrict__ ws) {
  extern __shared__ __attribute__((aligned(16))) char lds[];
  if (threadIdx.x < 16) ((u32*)(lds + K2_FLAG))[threadIdx.x] = 0u;
  wg_barrier();
  const int bid = blockIdx.x, xcd = bid & 7, idx = bid >> 3;
  if (__builtin_amdgcn_readfirstlane(threadIdx.x) < 256) {
    const int q = xcd & 3, b = (xcd >> 2) * 8 + (idx & 7), dq = idx >> 3;
    k2_gemm((const u32x4*)(ws + WS_H), (const u32x4*)(ws + WS_WD), (f32x4*)(ws + WS_PART), b, dq, q, lds);
  } else if (bid < 231) {
    k2_fft((const f32x2*)(ws + WS_P), (f32x2*)(ws + WS_R), w_r, w_i, bid, lds);
  }
}

NOPK __global__ __launch_bounds__(256) void k3_kernel(const char* __restrict__ ws, u16* __restrict__ out) {
  __shared__ __attribute__((aligned(16))) char lds[2 * 64 * 144];
  const int tid = threadIdx.x, w = __builtin_amdgcn_readfirstlane(tid >> 6), lane = tid & 63;
  const int bid = blockIdx.x, xcd = bid & 7, idx = bid >> 3;
  const int n2 = xcd * 4 + (idx & 3), j = idx >> 2;
  const int t = w >> 1, st = w & 1, r = lane & 31, hh = lane >> 5;
  const f32x2* R = (const f32x2*)(ws + WS_R) + (size_t)(n2 * 8 + j) * 7 * 33 * 8;
  const int k1 = tid >> 3, e = tid & 7;
  cf z[7];
  STAMP(0);
#pragma unroll
  for (int l = 0; l < 7; ++l) { f32x2 v = R[(l * 33 + k1) * 8 + e]; z[l] = {v[0], v[1]}; }
  if (k1 == 0) {
#pragma unroll
    for (int l = 0; l < 7; ++l) { f32x2 v = R[(l * 33 + 32) * 8 + e]; z[l] = {z[l].x - v[1], z[l].y + v[0]}; }
  }
  f32x4 bp[2][2];
  {
    const f32x4* part = (const f32x4*)(ws + WS_PART);
#pragma unroll
    for (int u = 0; u < 2; ++u)
#pragma unroll
      for (int v = 0; v < 2; ++v) {
        f32x4 s = {0.f, 0.f, 0.f, 0.f};
        if (4 * t + 2 * u + hh < 7) {
#pragma unroll
          for (int q = 0; q < 4; ++q) {
            size_t id = ((((((size_t)(q * 32 + n2) * 8 + j) * 2 + t) * 2 + st) * 2 + u) * 2 + v) * 64 + lane;
            s += part[id];
          }
        }
        bp[u][v] = s;
      }
  }
  const int n1 = 32 * st + r, s = 32 * n1 + n2;
  const float g = ((const float*)(ws + WS_GATE))[s];
  bf16x8 bt[4][2];
#pragma unroll
  for (int ks = 0; ks < 4; ++ks)
#pragma unroll
    for (int hl = 0; hl < 2; ++hl) bt[ks][hl] = *(const bf16x8*)&gT3.v[st][ks][hl][lane][0];
  {
    cf X0, A[3], B[3], y[7];
    dft7_ab(z, X0, A, B);
    y[0] = X0;
#pragma unroll
    for (int l = 0; l < 3; ++l) {
      y[l + 1] = {A[l].x - B[l].y, A[l].y + B[l].x};
      y[6 - l] = {A[l].x + B[l].y, A[l].y - B[l].x};
    }
#pragma unroll
    for (int m1 = 0; m1 < 7; ++m1) {
      const int rowi = 32 * (m1 >> 2) + 16 * ((m1 >> 1) & 1) + 8 * (e >> 2) + 4 * (m1 & 1) + (e & 3);
      u32 hi = pk_bf16(y[m1].x, y[m1].y);
      u32 lo = pk_bf16(y[m1].x - bflo(hi), y[m1].y - bfhi(hi));
      *(u32*)(lds + rowi * 144 + k1 * 4) = hi;
      *(u32*)(lds + 64 * 144 + rowi * 144 + k1 * 4) = lo;
    }
  }
  STAMP(1);
  wg_barrier();
  STAMP(2);
  f32x16 acc;
#pragma unroll
  for (int i = 0; i < 16; ++i) acc[i] = 0.f;
#pragma unroll
  for (int ks = 0; ks < 4; ++ks) {
    const char* ap = lds + (32 * t + r) * 144 + ks * 32 + hh * 16;
    bf16x8 ah = *(const bf16x8*)ap;
    bf16x8 al = *(const bf16x8*)(ap + 64 * 144);
    acc = mfma32(al, bt[ks][0], acc);
    acc = mfma32(ah, bt[ks][1], acc);
    acc = mfma32(ah, bt[ks][0], acc);
  }
  const float omg = rbf(1.0f - g);
#pragma unroll
  for (int u = 0; u < 2; ++u) {
    const int gseg = 4 * t + 2 * u + hh;
    if (gseg < 7) {
      u32x4 o;
#pragma unroll
      for (int v = 0; v < 2; ++v)
#pragma unroll
        for (int i2 = 0; i2 < 2; ++i2) {
          float r2[2];
#pragma unroll
          for (int k = 0; k < 2; ++k) {
            const int i = 2 * i2 + k;
            float a = rbf(acc[8 * u + 4 * v + i]);
            float b = rbf(bp[u][v][i]);
            r2[k] = rbf(g * a) + rbf(omg * b);
          }
          o[2 * v + i2] = pk_bf16(r2[0], r2[1]);
        }
      *(u32x4*)(out + (size_t)s * D_ + 64 * gseg + 8 * j) = o;
    }
  }
  STAMP(9);
}

extern "C" void kernel_launch(void* const* d_in, const int* in_sizes, int n_in, void* d_out, int out_size,
                              void* d_ws, size_t ws_size, hipStream_t stream) {
  const u16* x = (const u16*)d_in[0];
  const u16* w_r = (const u16*)d_in[1];
  const u16* w_i = (const u16*)d_in[2];
  const u16* wg = (const u16*)d_in[4];
  const u16* wu = (const u16*)d_in[5];
  const u16* wd = (const u16*)d_in[6];
  const u16* gw = (const u16*)d_in[7];
  char* ws = (char*)d_ws;
  (void)hipFuncSetAttribute((const void*)k1_kernel, hipFuncAttributeMaxDynamicSharedMemorySize, K1_LDS);
  (void)hipFuncSetAttribute((const void*)k2_kernel, hipFuncAttributeMaxDynamicSharedMemorySize, K2_LDS);
  const int g_kmask = 7;
  if (g_kmask & 1) k1_kernel<<<256, 512, K1_LDS, stream>>>(x, wg, wu, wd, gw, ws);
  if (g_kmask & 2) k2_kernel<<<256, 512, K2_LDS, stream>>>(w_r, w_i, ws);
  if (g_kmask & 4) k3_kernel<<<256, 256, 0, stream>>>(ws, (u16*)d_out);
}
```

```cpp
#include <hip/hip_runtime.h>
#include <hip/hip_bf16.h>

typedef unsigned short u16;
typedef unsigned int u32;
typedef __attribute__((ext_vector_type(8))) short bf16x8;
typedef __attribute__((ext_vector_type(4))) short s16x4;
typedef __attribute__((ext_vector_type(4))) float f32x4;
typedef __attribute__((ext_vector_type(2))) float f32x2;
typedef __attribute__((ext_vector_type(16))) float f32x16;
typedef __attribute__((ext_vector_type(4))) unsigned int u32x4;
typedef __attribute__((ext_vector_type(2))) unsigned int u32x2;
typedef __attribute__((ext_vector_type(2))) __bf16 bf16x2;

#if defined(__HIP_DEVICE_COMPILE__)
#define NOPK __attribute__((target("no-packed-fp32-ops")))
#else
#define NOPK
#endif
#define DEV NOPK __device__ __forceinline__

constexpr int S_ = 2048, D_ = 448, F_ = 1792, DF_ = 225;

constexpr double kPi = 3.14159265358979323846264338327950288;
constexpr double tsin(double x) {
  double t = x, s = x;
  for (int i = 1; i < 12; ++i) { t *= -x * x / ((2 * i) * (2 * i + 1)); s += t; }
  return s;
}
constexpr double tcos(double x) {
  double t = 1, s = 1;
  for (int i = 1; i < 12; ++i) { t *= -x * x / ((2 * i - 1) * (2 * i)); s += t; }
  return s;
}
constexpr double cosn(long n, long N) {
  n %= N; if (n < 0) n += N;
  if (2 * n > N) n = N - n;
  if (4 * n > N) return -cosn(N - 2 * n, 2 * N) ;
  if (8 * n > N) return tsin(2 * kPi * (double)(N - 4 * n) / (double)(4 * N));
  return tcos(2 * kPi * (double)n / (double)N);
}
constexpr double sinn(long n, long N) { return cosn(4 * n - N, 4 * N); }

constexpr u16 f2bf(float f) {
  u32 u = __builtin_bit_cast(u32, f);
  return (u16)((u + 0x7FFFu + ((u >> 16) & 1u)) >> 16);
}
constexpr float bf2f(u16 h) { return __builtin_bit_cast(float, (u32)h << 16); }

struct C64 { double c[64], s[64]; };
constexpr C64 mkc64() { C64 t{}; for (int i = 0; i < 64; ++i) { t.c[i] = cosn(i, 64); t.s[i] = sinn(i, 64); } return t; }
constexpr C64 c64 = mkc64();

struct FragTab { u16 v[2][4][2][64][8]; };
constexpr FragTab mkT1() {
  FragTab t{};
  for (int kt = 0; kt < 4; ++kt) for (int ks = 0; ks < 2; ++ks) for (int l = 0; l < 64; ++l) for (int j = 0; j < 8; ++j) {
    int kap = 16 * kt + (l & 15), k1 = kap >> 1, part = kap & 1;
    int n1 = 32 * ks + 8 * (l >> 4) + j;
    double val = part ? -c64.s[(n1 * k1) & 63] : c64.c[(n1 * k1) & 63];
    if (kap == 1) val = (n1 & 1) ? -1.0 : 1.0;
    u16 hi = f2bf((float)val);
    u16 lo = f2bf((float)(val - (double)bf2f(hi)));
    t.v[kt >> 1][(kt & 1) * 2 + ks][0][l][j] = hi; t.v[kt >> 1][(kt & 1) * 2 + ks][1][l][j] = lo;
  }
  return t;
}
constexpr FragTab mkT3() {
  FragTab t{};
  for (int st = 0; st < 2; ++st) for (int ks = 0; ks < 4; ++ks) for (int l = 0; l < 64; ++l) for (int j = 0; j < 8; ++j) {
    int n1 = 32 * st + (l & 31), kap = 16 * ks + 8 * (l >> 5) + j;
    int k1 = kap >> 1, part = kap & 1;
    double val = part ? -2.0 * c64.s[(n1 * k1) & 63] : 2.0 * c64.c[(n1 * k1) & 63];
    if (k1 == 0) val = part ? ((n1 & 1) ? -1.0 : 1.0) : 1.0;
    val /= (double)(S_ * D_);
    u16 hi = f2bf((float)val);
    u16 lo = f2bf((float)(val - (double)bf2f(hi)));
    t.v[st][ks][0][l][j] = hi; t.v[st][ks][1][l][j] = lo;
  }
  return t;
}
struct Tw2048 { float v[2048][2]; };
struct Tw448 { float v[448][2]; };
constexpr Tw2048 mkTw2048() { Tw2048 t{}; for (int i = 0; i < 2048; ++i) { t.v[i][0] = (float)cosn(i, 2048); t.v[i][1] = (float)-sinn(i, 2048); } return t; }
constexpr Tw448 mkTw448() { Tw448 t{}; for (int i = 0; i < 448; ++i) { t.v[i][0] = (float)cosn(i, 448); t.v[i][1] = (float)-sinn(i, 448); } return t; }

struct Twb { float v[7][4][8][4]; };
constexpr Twb mkTwb() {
  Twb t{};
  for (int l1 = 0; l1 < 7; ++l1) for (int al = 0; al < 8; ++al) for (int b = 0; b < 8; ++b) {
    t.v[l1][al >> 1][b][(al & 1) * 2] = (float)cosn(b * (7 * al + l1), 448);
    t.v[l1][al >> 1][b][(al & 1) * 2 + 1] = (float)-sinn(b * (7 * al + l1), 448);
  }
  return t;
}
static __constant__ __attribute__((aligned(16))) Twb gTwb = mkTwb();
static __constant__ __attribute__((aligned(16))) FragTab gT1 = mkT1();
static __constant__ __attribute__((aligned(16))) FragTab gT3 = mkT3();
static __constant__ __attribute__((aligned(16))) Tw2048 gTw2048 = mkTw2048();
static __constant__ __attribute__((aligned(16))) Tw448 gTw448 = mkTw448();

constexpr float C7_1 = (float)cosn(1, 7), C7_2 = (float)cosn(2, 7), C7_3 = (float)cosn(3, 7);
constexpr float S7_1 = (float)sinn(1, 7), S7_2 = (float)sinn(2, 7), S7_3 = (float)sinn(3, 7);

DEV u32 pk_bf16(float a, float b) { bf16x2 v = {(__bf16)a, (__bf16)b}; return __builtin_bit_cast(u32, v); }
DEV float bflo(u32 v) { return __builtin_bit_cast(float, v << 16); }
DEV float bfhi(u32 v) { return __builtin_bit_cast(float, v & 0xffff0000u); }
DEV float rbf(float a) { return (float)(__bf16)a; }
DEV u32 perm_lo(u32 hi_src, u32 lo_src) { return __builtin_amdgcn_perm(hi_src, lo_src, 0x05040100u); }
DEV u32 perm_hi(u32 hi_src, u32 lo_src) { return __builtin_amdgcn_perm(hi_src, lo_src, 0x07060302u); }
DEV bf16x8 as_frag(u32x4 v) { return __builtin_bit_cast(bf16x8, v); }
DEV f32x4 mfma16(bf16x8 a, bf16x8 b, f32x4 c) { return __builtin_amdgcn_mfma_f32_16x16x32_bf16(a, b, c, 0, 0, 0); }
DEV f32x16 mfma32(bf16x8 a, bf16x8 b, f32x16 c) { return __builtin_amdgcn_mfma_f32_32x32x16_bf16(a, b, c, 0, 0, 0); }

DEV void wg_barrier() {
  __builtin_amdgcn_fence(__ATOMIC_RELEASE, "workgroup");
  __builtin_amdgcn_s_barrier();
  __builtin_amdgcn_fence(__ATOMIC_ACQUIRE, "workgroup");
}

struct cf { float x, y; };
DEV cf cadd(cf a, cf b) { return {a.x + b.x, a.y + b.y}; }
DEV cf csub(cf a, cf b) { return {a.x - b.x, a.y - b.y}; }
DEV cf cmul(cf a, cf b) { return {a.x * b.x - a.y * b.y, a.x * b.y + a.y * b.x}; }
DEV cf cmulc(cf a, cf b) { return {a.x * b.x + a.y * b.y, a.y * b.x - a.x * b.y}; }
template <int INV> DEV cf ctw(cf a, cf w) { return INV ? cmulc(a, w) : cmul(a, w); }
template <int INV> DEV cf rot90(cf a) { return INV ? cf{-a.y, a.x} : cf{a.y, -a.x}; }

template <int INV> DEV void dft4(cf& a0, cf& a1, cf& a2, cf& a3) {
  cf t0 = cadd(a0, a2), t1 = csub(a0, a2), t2 = cadd(a1, a3), t3 = rot90<INV>(csub(a1, a3));
  a0 = cadd(t0, t2); a2 = csub(t0, t2); a1 = cadd(t1, t3); a3 = csub(t1, t3);
}
template <int INV> DEV void dft8(cf (&v)[8]) {
  constexpr float R = 0.70710678118654752440f;
  cf e0 = v[0], e1 = v[2], e2 = v[4], e3 = v[6], o0 = v[1], o1 = v[3], o2 = v[5], o3 = v[7];
  dft4<INV>(e0, e1, e2, e3);
  dft4<INV>(o0, o1, o2, o3);
  cf p1 = INV ? cf{(o1.x - o1.y) * R, (o1.x + o1.y) * R} : cf{(o1.x + o1.y) * R, (o1.y - o1.x) * R};
  cf p2 = rot90<INV>(o2);
  cf p3 = INV ? cf{(-o3.x - o3.y) * R, (o3.x - o3.y) * R} : cf{(o3.y - o3.x) * R, (-o3.x - o3.y) * R};
  v[0] = cadd(e0, o0); v[4] = csub(e0, o0);
  v[1] = cadd(e1, p1); v[5] = csub(e1, p1);
  v[2] = cadd(e2, p2); v[6] = csub(e2, p2);
  v[3] = cadd(e3, p3); v[7] = csub(e3, p3);
}
DEV void dft7_ab(const cf (&z)[7], cf& X0, cf (&A)[3], cf (&B)[3]) {
  cf t1 = cadd(z[1], z[6]), t2 = cadd(z[2], z[5]), t3 = cadd(z[3], z[4]);
  cf s1 = csub(z[1], z[6]), s2 = csub(z[2], z[5]), s3 = csub(z[3], z[4]);
  X0 = {z[0].x + t1.x + t2.x + t3.x, z[0].y + t1.y + t2.y + t3.y};
  A[0] = {z[0].x + C7_1 * t1.x + C7_2 * t2.x + C7_3 * t3.x, z[0].y + C7_1 * t1.y + C7_2 * t2.y + C7_3 * t3.y};
  A[1] = {z[0].x + C7_2 * t1.x + C7_3 * t2.x + C7_1 * t3.x, z[0].y + C7_2 * t1.y + C7_3 * t2.y + C7_1 * t3.y};
  A[2] = {z[0].x + C7_3 * t1.x + C7_1 * t2.x + C7_2 * t3.x, z[0].y + C7_3 * t1.y + C7_1 * t2.y + C7_2 * t3.y};
  B[0] = {S7_1 * s1.x + S7_2 * s2.x + S7_3 * s3.x, S7_1 * s1.y + S7_2 * s2.y + S7_3 * s3.y};
  B[1] = {S7_2 * s1.x - S7_3 * s2.x - S7_1 * s3.x, S7_2 * s1.y - S7_3 * s2.y - S7_1 * s3.y};
  B[2] = {S7_3 * s1.x - S7_1 * s2.x + S7_2 * s3.x, S7_3 * s1.y - S7_1 * s2.y + S7_2 * s3.y};
}

DEV float gelu_mul(float g, float u) {
  const float c1 = -2.0f * 1.4426950408889634f * 0.7978845608028654f;
  const float c2 = c1 * 0.044715f;
  float a = g * __builtin_fmaf(g * g, c2, c1);
  float e = __builtin_amdgcn_exp2f(a);
  float r = __builtin_amdgcn_rcpf(1.0f + e);
  return (g * u) * r;
}

constexpr int OFF_E = 0;
constexpr int OFF_TMO = 256;
constexpr int OFF_CNT = 4096;
constexpr int OFF_HB = 8192;
constexpr int OFF_RB = 12288;
constexpr int OFF_QF = 49152;
constexpr int OFF_GF = 32768;
constexpr int OFF_WF = 20480;
constexpr int OFF_PF = 16384;
constexpr int WS_H = 65536;
constexpr int WS_WD = WS_H + S_ * F_ * 2;
constexpr int WS_P = WS_WD + F_ * D_ * 2;
constexpr int WS_R = WS_P + 17 * 7 * 2048 * 16;
constexpr int WS_GATE = WS_R + 231 * 2048 * 8;
constexpr int WS_WF = WS_GATE + 2048 * 4;
constexpr int WS_END = WS_WF + 231 * 8192;

constexpr int L_X = 0;
constexpr int L_W = 98304;
constexpr int L_BP = 0;
constexpr int L_F = L_W + 5 * 8192;
constexpr int L_XP = L_F;
constexpr int L_U = L_F + 8192;
constexpr int L_FA = L_F;
constexpr int L_INV = L_F;
constexpr int L_AG = 61440;
constexpr int L_CTL = L_F + 23296;
constexpr int LDS_BYTES = L_CTL + 256;
static_assert(LDS_BYTES <= 163840, "LDS");
constexpr int C_QGO_ = 43;
constexpr int C_W = 0;
constexpr int C_G = 8;
constexpr int C_RARR = 16;
constexpr int C_FIN = 17;
constexpr int C_FFT = 20;
constexpr int C_INV = 24;
constexpr int C_B = 44;
constexpr int C_FS = 28;
constexpr int C_ADONE = 32;
constexpr int C_PCNT = 34;
constexpr int C_RCNT = 35;
constexpr int C_RGO = 36;
constexpr int C_GCNT = 37;
constexpr int C_WCNT = 18;
constexpr int C_WGO = C_QGO_;
constexpr int C_PGO = 38;
constexpr int C_QCNT = 39;
constexpr int C_QGO = 43;
constexpr int C_TAG = 40;

#define LAS __attribute__((address_space(3)))
typedef LAS volatile u32 lds_vu32;
typedef LAS volatile u32x4 lds_vu32x4;
typedef unsigned long long u64;
typedef __amdgpu_buffer_rsrc_t rsrc_t;
constexpr int AUX_SC1 = 16;
#define FPRIO 2
#define GDELAY 0
#define FSTART 6u
#define QSTART 12u
#define CODE_KB 32
#define EPRIO 3
#define WSTART 9u
#define ROT1 1
#define ROT2 1
#define LSLEEP 2
constexpr int SPIN_LDS = 1 << 18;
constexpr int SPIN_GLB = 1 << 14;

#define DRAIN() asm volatile("s_waitcnt vmcnt(0)" ::: "memory")
#define SB0() __builtin_amdgcn_sched_barrier(0)
#define WFENCE() __builtin_amdgcn_fence(__ATOMIC_ACQ_REL, "wavefront")

DEV u32x4 ld128s(rsrc_t rs, int off) { return __builtin_amdgcn_raw_buffer_load_b128(rs, off, 0, AUX_SC1); }
DEV u32x2 ld64s(rsrc_t rs, int off) { return __builtin_amdgcn_raw_buffer_load_b64(rs, off, 0, AUX_SC1); }
DEV u32 ld32s(rsrc_t rs, int off) { return __builtin_amdgcn_raw_buffer_load_b32(rs, off, 0, AUX_SC1); }
DEV void st128s(rsrc_t rs, int off, u32x4 v) { __builtin_amdgcn_raw_buffer_store_b128(v, rs, off, 0, AUX_SC1); }
DEV void st64s(rsrc_t rs, int off, u32x2 v) { __builtin_amdgcn_raw_buffer_store_b64(v, rs, off, 0, AUX_SC1); }
DEV void st32s(rsrc_t rs, int off, u32 v) { __builtin_amdgcn_raw_buffer_store_b32(v, rs, off, 0, AUX_SC1); }
DEV void stc(rsrc_t rs, int off, float x, float y) { st64s(rs, off, u32x2{__builtin_bit_cast(u32, x), __builtin_bit_cast(u32, y)}); }
DEV cf ldc(rsrc_t rs, int off) { f32x2 v = __builtin_bit_cast(f32x2, ld64s(rs, off)); return cf{v[0], v[1]}; }

DEV void lds_set(LAS char* l, int word, u32 v, int lane) {
  __builtin_amdgcn_fence(__ATOMIC_RELEASE, "workgroup");
  if (lane == 0) *(lds_vu32*)(l + L_CTL + word * 4) = v;
}
DEV void lds_wait1(LAS char* l, int word, u32 v) {
  _Pragma("unroll 1") for (int spin = 0; spin < SPIN_LDS; ++spin) {
    u32 f = *(lds_vu32*)(l + L_CTL + word * 4);
    if ((u32)__builtin_amdgcn_readfirstlane(f) >= v) break;
    __builtin_amdgcn_s_sleep(LSLEEP);
  }
  __builtin_amdgcn_fence(__ATOMIC_ACQUIRE, "workgroup");
}
DEV u32 min4(u32x4 f) { u32 a = f[0] < f[1] ? f[0] : f[1], b = f[2] < f[3] ? f[2] : f[3]; return a < b ? a : b; }
DEV void lds_wait4(LAS char* l, int word, u32 v) {
  _Pragma("unroll 1") for (int spin = 0; spin < SPIN_LDS; ++spin) {
    u32x4 f = *(lds_vu32x4*)(l + L_CTL + word * 4);
    if ((u32)__builtin_amdgcn_readfirstlane(min4(f)) >= v) break;
    __builtin_amdgcn_s_sleep(LSLEEP);
  }
  __builtin_amdgcn_fence(__ATOMIC_ACQUIRE, "workgroup");
}
DEV void lds_wait4_tight(LAS char* l, int word, u32 v) {
  _Pragma("unroll 1") for (int spin = 0; spin < SPIN_LDS; ++spin) {
    u32x4 f = *(lds_vu32x4*)(l + L_CTL + word * 4);
    if ((u32)__builtin_amdgcn_readfirstlane(min4(f)) >= v) break;
  }
}
DEV void lds_wait8(LAS char* l, int word, u32 v) {
  _Pragma("unroll 1") for (int spin = 0; spin < SPIN_LDS; ++spin) {
    u32x4 f = *(lds_vu32x4*)(l + L_CTL + word * 4);
    u32x4 g = *(lds_vu32x4*)(l + L_CTL + word * 4 + 16);
    u32 a = min4(f), b = min4(g);
    if ((u32)__builtin_amdgcn_readfirstlane(a < b ? a : b) >= v) break;
    __builtin_amdgcn_s_sleep(LSLEEP);
  }
  __builtin_amdgcn_fence(__ATOMIC_ACQUIRE, "workgroup");
}
DEV u32 lds_arrive(LAS char* l, int word, int lane) {
  __builtin_amdgcn_fence(__ATOMIC_RELEASE, "workgroup");
  u32 old = 0;
  if (lane == 0) old = __hip_atomic_fetch_add((LAS u32*)(l + L_CTL + word * 4), 1u, __ATOMIC_RELAXED, __HIP_MEMORY_SCOPE_WORKGROUP);
  return (u32)__builtin_amdgcn_readfirstlane(old);
}
DEV u32x2 get_tag(LAS char* l) {
  lds_wait1(l, C_TAG + 2, 1u);
  u32 lo = *(lds_vu32*)(l + L_CTL + C_TAG * 4), hi = *(lds_vu32*)(l + L_CTL + C_TAG * 4 + 4);
  return u32x2{(u32)__builtin_amdgcn_readfirstlane(lo), (u32)__builtin_amdgcn_readfirstlane(hi)};
}
DEV void put_tag(LAS char* l, u32x2 ep, int w, int lane) {
  if (w == 7) {
    u64 tg = (((u64)ep[1] << 32) | ep[0]) + 1ull;
    if (lane == 0) { *(lds_vu32*)(l + L_CTL + C_TAG * 4) = (u32)tg; *(lds_vu32*)(l + L_CTL + C_TAG * 4 + 4) = (u32)(tg >> 32); }
    lds_set(l, C_TAG + 2, 1u, lane);
  }
}
DEV bool tag_ok2(u32x4 v, u32x2 tag) { return v[0] == tag[0] && v[1] == tag[1] && v[2] == tag[0] && v[3] == tag[1]; }
DEV void poll256(rsrc_t rs, int off, u32x2 tag, int lane) {
  _Pragma("unroll 1") for (int spin = 0; spin < SPIN_GLB; ++spin) {
    u32x4 a = ld128s(rs, off + lane * 16), b = ld128s(rs, off + 1024 + lane * 16);
    bool ok = tag_ok2(a, tag) && tag_ok2(b, tag);
    if (__builtin_amdgcn_ballot_w64(!ok) == 0ull) { __builtin_amdgcn_fence(__ATOMIC_ACQUIRE, "wavefront"); return; }
    __builtin_amdgcn_s_sleep(8);
  }
  if (lane == 0) st32s(rs, OFF_TMO, 1u);
}
DEV u32 glb_arrive(char* ws, int idx, int lane) {
  u32 old = 0;
  if (lane == 0) old = __hip_atomic_fetch_add((__attribute__((address_space(1))) u32*)(ws + OFF_CNT + idx * 128), 1u, __ATOMIC_RELAXED, __HIP_MEMORY_SCOPE_AGENT);
  return (u32)__builtin_amdgcn_readfirstlane(old);
}
DEV bool flag_ok(u32x2 v, u32x2 tag, bool active) { return __builtin_amdgcn_ballot_w64(active && !(v[0] == tag[0] && v[1] == tag[1])) == 0ull; }
DEV void poll_lanes(rsrc_t rs, int off, bool active, u32x2 tag, int lane) {
  _Pragma("unroll 1") for (int spin = 0; spin < SPIN_GLB; ++spin) {
    u32x2 v = ld64s(rs, off);
    if (flag_ok(v, tag, active)) { __builtin_amdgcn_fence(__ATOMIC_ACQUIRE, "wavefront"); return; }
    __builtin_amdgcn_s_sleep(2);
  }
  if (lane == 0) st32s(rs, OFF_TMO, 2u);
}

constexpr int AXS = 912;

template <int LOFF, int SOFF> DEV void bdma(rsrc_t r, u32 voff, u32 lbase) {
  u32 tmp;
  asm volatile("s_add_u32 m0, %3, %4\n\ts_mov_b32 %0, %5\n\tbuffer_load_dwordx4 %1, %2, %0 offen lds"
               : "=&s"(tmp) : "v"(voff), "s"(r), "s"(lbase), "i"(LOFF), "i"(SOFF) : "memory", "scc");
}
template <int LOFF, int SOFF> DEV void bdma_b(rsrc_t r, u32 voff, u32 lbase, u32 sbase) {
  u32 tmp;
  asm volatile("s_add_u32 m0, %3, %4\n\ts_add_u32 %0, %6, %5\n\tbuffer_load_dwordx4 %1, %2, %0 offen lds"
               : "=&s"(tmp) : "v"(voff), "s"(r), "s"(lbase), "i"(LOFF), "i"(SOFF), "s"(sbase) : "memory", "scc");
}
template <int LOFF, int SOFF> DEV void bdma_sc1b(rsrc_t r, u32 voff, u32 lbase, u32 sbase) {
  u32 tmp;
  asm volatile("s_add_u32 m0, %3, %4\n\ts_add_u32 %0, %6, %5\n\tbuffer_load_dwordx4 %1, %2, %0 offen sc1 lds"
               : "=&s"(tmp) : "v"(voff), "s"(r), "s"(lbase), "i"(LOFF), "i"(SOFF), "s"(sbase) : "memory", "scc");
}
template <int LOFF, int SOFF> DEV void bdma_sc1(rsrc_t r, u32 voff, u32 lbase) {
  u32 tmp;
  asm volatile("s_add_u32 m0, %3, %4\n\ts_mov_b32 %0, %5\n\tbuffer_load_dwordx4 %1, %2, %0 offen sc1 lds"
               : "=&s"(tmp) : "v"(voff), "s"(r), "s"(lbase), "i"(LOFF), "i"(SOFF) : "memory", "scc");
}
DEV void ffn1_core(const u16* __restrict__ x, const u16* __restrict__ wg, const u16* __restrict__ wu, int rn2, int fb,
                   LAS char* lds3, int wl, int lane, f32x16 (&acc)[2][2][2], rsrc_t rs, int wo0, int wo1x, u32& qrdy, int rot) {
  const u32 lbase = (u32)(size_t)lds3;
  rsrc_t rx = __builtin_amdgcn_make_buffer_rsrc((void*)x, 0, S_ * D_ * 2, 0x00020000);
  rsrc_t rw = __builtin_amdgcn_make_buffer_rsrc((void*)((wl & 1) ? wu : wg), 0, D_ * F_ * 2, 0x00020000);
  const u32 xv0 = (u32)((((lane >> 3) * 32 + rn2) * D_) * 2 + (((lane & 7) ^ (lane >> 4)) * 16));
  const u32 xv1 = xv0 ^ 64u;
  const u32 xld = lbase + L_X + wl * 8192;
  const int pp = lane & 7, b1 = (lane >> 3) & 3;
  const u32 wv0 = (u32)((((wl >> 1) * 16 + (lane >> 3)) * F_ + 64 * fb) * 2 + ((((pp >> 1) ^ b1) * 2 + (pp & 1)) * 16));
  const u32 wv1 = (wv0 ^ 32u) + 8 * F_ * 2;
  const u32 wld = lbase + L_W + (wl & 1) * 4096 + (wl >> 1) * 2048;
#define RC(c) ((u32)((c) + rot >= 7 ? (c) + rot - 7 : (c) + rot))
#define XD1(c, i) bdma_b<((c) % 3) * 32768 + (i) * 1024, (i) * (8 * 32 * D_ * 2)>(rx, ((i) & 1) ? xv1 : xv0, xld, RC(c) * 128u)
#define WD1(s, i) bdma_b<((s) % 5) * 8192 + (i) * 1024, ((s) & 1) * 32 * F_ * 2>(rw, (i) ? wv1 : wv0, wld, RC((s) >> 1) * (u32)(64 * F_ * 2))
#define PRO_BARRIER() do { __builtin_amdgcn_s_barrier(); } while (0)
#define VMWAIT(n) asm volatile("s_waitcnt vmcnt(" #n ")" ::: "memory")
  lds_vu32* pubp = (lds_vu32*)(lds3 + L_CTL + (C_W + wl) * 4);
#define PUB(v) do { *pubp = (u32)(v); } while (0)
#define RDFLAG() do { flg = *(lds_vu32x4*)(lds3 + L_CTL + C_W * 4); } while (0)
#define FLGMIN() do { fmin = (u32)__builtin_amdgcn_readfirstlane(min4(flg)); } while (0)
#define CHKFLAG(v) do { if (__builtin_expect(fmin < (u32)(v), 0)) lds_wait4_tight(lds3, C_W, (u32)(v)); \
                        __builtin_amdgcn_fence(__ATOMIC_ACQUIRE, "workgroup"); } while (0)
  const int r32 = lane & 31, hh = lane >> 5;
  LAS char* xr[4];
#pragma unroll
  for (int k = 0; k < 4; ++k) xr[k] = lds3 + L_X + wl * 8192 + r32 * 128 + (((2 * k + hh) ^ ((r32 >> 1) & 7)) * 16);
  const int blk = (lane >> 4) & 1, qq = (lane & 15) >> 2, p4 = lane & 3;
  LAS char* wt[2];
#pragma unroll
  for (int ph = 0; ph < 2; ++ph) wt[ph] = lds3 + L_W + (8 * hh + qq) * 128 + (((2 * ph + blk) ^ qq ^ hh) * 32) + 8 * p4;
#define WOFF(P, m) ((((P) >> 1) % 5) * 8192 + (m) * 4096 + ((P) & 1) * 2048)
#define LDA(P, m, ph) afr[(P) & 1][m][ph] = __builtin_shufflevector( \
      __builtin_amdgcn_ds_read_tr16_b64_v4i16((LAS s16x4*)(wt[ph] + WOFF(P, m))), \
      __builtin_amdgcn_ds_read_tr16_b64_v4i16((LAS s16x4*)(wt[ph] + WOFF(P, m) + 512)), 0, 1, 2, 3, 4, 5, 6, 7)
#define LDB(P, Z) bfr[(P) & 1][Z] = *(LAS bf16x8*)(xr[(P) & 3] + (((P) >> 2) % 3) * 32768 + (Z) * 4096)
#define MM(P, m, ph, Z) do { acc[m][ph][Z] = mfma32(afr[(P) & 1][m][ph], bfr[(P) & 1][Z], acc[m][ph][Z]); SB0(); } while (0)
  bf16x8 bfr[2][2], afr[2][2][2];
  u32x4 flg;
  u32 fmin;
#pragma unroll
  for (int a = 0; a < 8; ++a)
#pragma unroll
    for (int i = 0; i < 16; ++i) acc[a >> 2][(a >> 1) & 1][a & 1][i] = 0.f;
  XD1(0, 0); XD1(0, 1); XD1(0, 2); XD1(0, 3); XD1(0, 4); XD1(0, 5); XD1(0, 6); XD1(0, 7); WD1(0, 0); WD1(0, 1); WD1(1, 0); WD1(1, 1); XD1(1, 0); XD1(1, 1); XD1(1, 2); XD1(1, 3); XD1(1, 4); XD1(1, 5); XD1(1, 6); XD1(1, 7); WD1(2, 0); WD1(2, 1); WD1(3, 0); WD1(3, 1); SB0();
  PRO_BARRIER();
  VMWAIT(14); PUB(1); lds_wait4(lds3, C_W, 1u);
  LDB(0, 0); LDA(0, 0, 0); LDB(0, 1); LDA(0, 0, 1); LDA(0, 1, 0); LDA(0, 1, 1); SB0();
  VMWAIT(12); PUB(2); SB0();
  MM(0, 0, 0, 0); LDB(1, 0); XD1(2, 0); SB0();
  MM(0, 0, 0, 1); LDA(1, 0, 0); SB0();
  MM(0, 0, 1, 0); LDB(1, 1); RDFLAG(); XD1(2, 1); SB0();
  MM(0, 0, 1, 1); LDA(1, 0, 1); SB0();
  MM(0, 1, 0, 0); LDA(1, 1, 0); XD1(2, 2); SB0();
  MM(0, 1, 0, 1); LDA(1, 1, 1); SB0();
  MM(0, 1, 1, 0); FLGMIN(); XD1(2, 3); SB0();
  MM(0, 1, 1, 1); XD1(2, 4); SB0();
  CHKFLAG(2); SB0();
  MM(1, 0, 0, 0); LDB(2, 0); WD1(4, 0); SB0();
  MM(1, 0, 0, 1); LDA(2, 0, 0); VMWAIT(8); PUB(3); SB0();
  MM(1, 0, 1, 0); LDB(2, 1); WD1(4, 1); SB0();
  MM(1, 0, 1, 1); LDA(2, 0, 1); SB0();
  MM(1, 1, 0, 0); LDA(2, 1, 0); XD1(2, 5); SB0();
  MM(1, 1, 0, 1); LDA(2, 1, 1); SB0();
  MM(1, 1, 1, 0); XD1(2, 6); SB0();
  MM(1, 1, 1, 1); XD1(2, 7); SB0();
  MM(2, 0, 0, 0); LDB(3, 0); SB0();
  MM(2, 0, 0, 1); LDA(3, 0, 0); SB0();
  MM(2, 0, 1, 0); LDB(3, 1); RDFLAG(); SB0();
  MM(2, 0, 1, 1); LDA(3, 0, 1); SB0();
  MM(2, 1, 0, 0); LDA(3, 1, 0); SB0();
  MM(2, 1, 0, 1); LDA(3, 1, 1); SB0();
  MM(2, 1, 1, 0); FLGMIN(); SB0();
  MM(2, 1, 1, 1);  SB0();
  CHKFLAG(3); SB0();
  MM(3, 0, 0, 0); LDB(4, 0); WD1(5, 0); SB0();
  MM(3, 0, 0, 1); LDA(4, 0, 0); VMWAIT(11); PUB(4); SB0();
  MM(3, 0, 1, 0); LDB(4, 1); WD1(5, 1); SB0();
  MM(3, 0, 1, 1); LDA(4, 0, 1); SB0();
  MM(3, 1, 0, 0); LDA(4, 1, 0); XD1(3, 0); SB0();
  MM(3, 1, 0, 1); LDA(4, 1, 1); SB0();
  MM(3, 1, 1, 0); XD1(3, 1); SB0();
  MM(3, 1, 1, 1); XD1(3, 2); SB0();
  MM(4, 0, 0, 0); LDB(5, 0); XD1(3, 3); SB0();
  MM(4, 0, 0, 1); LDA(5, 0, 0); SB0();
  MM(4, 0, 1, 0); LDB(5, 1); RDFLAG(); XD1(3, 4); SB0();
  MM(4, 0, 1, 1); LDA(5, 0, 1); SB0();
  MM(4, 1, 0, 0); LDA(5, 1, 0); XD1(3, 5); SB0();
  MM(4, 1, 0, 1); LDA(5, 1, 1); SB0();
  MM(4, 1, 1, 0); FLGMIN(); XD1(3, 6); SB0();
  MM(4, 1, 1, 1); XD1(3, 7); SB0();
  CHKFLAG(4); SB0();
  MM(5, 0, 0, 0); LDB(6, 0); WD1(6, 0); SB0();
  MM(5, 0, 0, 1); LDA(6, 0, 0); VMWAIT(11); PUB(5); SB0();
  MM(5, 0, 1, 0); LDB(6, 1); WD1(6, 1); SB0();
  MM(5, 0, 1, 1); LDA(6, 0, 1); SB0();
  MM(5, 1, 0, 0); LDA(6, 1, 0); SB0();
  MM(5, 1, 0, 1); LDA(6, 1, 1); SB0();
  MM(5, 1, 1, 0);  SB0();
  MM(5, 1, 1, 1);  SB0();
  MM(6, 0, 0, 0); LDB(7, 0); SB0();
  MM(6, 0, 0, 1); LDA(7, 0, 0); SB0();
  MM(6, 0, 1, 0); LDB(7, 1); RDFLAG(); SB0();
  MM(6, 0, 1, 1); LDA(7, 0, 1); SB0();
  MM(6, 1, 0, 0); LDA(7, 1, 0); SB0();
  MM(6, 1, 0, 1); LDA(7, 1, 1); SB0();
  MM(6, 1, 1, 0); FLGMIN(); SB0();
  MM(6, 1, 1, 1);  SB0();
  CHKFLAG(5); SB0();
  MM(7, 0, 0, 0); LDB(8, 0); WD1(7, 0); SB0();
  MM(7, 0, 0, 1); LDA(8, 0, 0); VMWAIT(11); PUB(6); SB0();
  MM(7, 0, 1, 0); LDB(8, 1); WD1(7, 1); SB0();
  MM(7, 0, 1, 1); LDA(8, 0, 1); SB0();
  MM(7, 1, 0, 0); LDA(8, 1, 0); XD1(4, 0); SB0();
  MM(7, 1, 0, 1); LDA(8, 1, 1); SB0();
  MM(7, 1, 1, 0); XD1(4, 1); SB0();
  MM(7, 1, 1, 1); XD1(4, 2); SB0();
  MM(8, 0, 0, 0); LDB(9, 0); XD1(4, 3); SB0();
  MM(8, 0, 0, 1); LDA(9, 0, 0); SB0();
  MM(8, 0, 1, 0); LDB(9, 1); RDFLAG(); XD1(4, 4); SB0();
  MM(8, 0, 1, 1); LDA(9, 0, 1); SB0();
  MM(8, 1, 0, 0); LDA(9, 1, 0); XD1(4, 5); SB0();
  MM(8, 1, 0, 1); LDA(9, 1, 1); SB0();
  MM(8, 1, 1, 0); FLGMIN(); XD1(4, 6); SB0();
  MM(8, 1, 1, 1); XD1(4, 7); SB0();
  CHKFLAG(6); SB0();
  MM(9, 0, 0, 0); LDB(10, 0); WD1(8, 0); SB0();
  MM(9, 0, 0, 1); LDA(10, 0, 0); VMWAIT(11); PUB(7); SB0();
  MM(9, 0, 1, 0); LDB(10, 1); WD1(8, 1); SB0();
  MM(9, 0, 1, 1); LDA(10, 0, 1); SB0();
  MM(9, 1, 0, 0); LDA(10, 1, 0); SB0();
  MM(9, 1, 0, 1); LDA(10, 1, 1); SB0();
  MM(9, 1, 1, 0);  SB0();
  MM(9, 1, 1, 1);  SB0();
  MM(10, 0, 0, 0); LDB(11, 0); SB0();
  MM(10, 0, 0, 1); LDA(11, 0, 0); SB0();
  MM(10, 0, 1, 0); LDB(11, 1); RDFLAG(); SB0();
  MM(10, 0, 1, 1); LDA(11, 0, 1); SB0();
  MM(10, 1, 0, 0); LDA(11, 1, 0); SB0();
  MM(10, 1, 0, 1); LDA(11, 1, 1); SB0();
  MM(10, 1, 1, 0); FLGMIN(); SB0();
  MM(10, 1, 1, 1);  SB0();
  CHKFLAG(7); SB0();
  MM(11, 0, 0, 0); LDB(12, 0); WD1(9, 0); SB0();
  MM(11, 0, 0, 1); LDA(12, 0, 0); VMWAIT(11); PUB(8); SB0();
  MM(11, 0, 1, 0); LDB(12, 1); WD1(9, 1); SB0();
  MM(11, 0, 1, 1); LDA(12, 0, 1); SB0();
  MM(11, 1, 0, 0); LDA(12, 1, 0); XD1(5, 0); SB0();
  MM(11, 1, 0, 1); LDA(12, 1, 1); SB0();
  MM(11, 1, 1, 0); XD1(5, 1); SB0();
  MM(11, 1, 1, 1); XD1(5, 2); SB0();
  MM(12, 0, 0, 0); LDB(13, 0); XD1(5, 3); SB0();
  MM(12, 0, 0, 1); LDA(13, 0, 0); SB0();
  MM(12, 0, 1, 0); LDB(13, 1); RDFLAG(); XD1(5, 4); SB0();
  MM(12, 0, 1, 1); LDA(13, 0, 1); SB0();
  MM(12, 1, 0, 0); LDA(13, 1, 0); XD1(5, 5); SB0();
  MM(12, 1, 0, 1); LDA(13, 1, 1); SB0();
  MM(12, 1, 1, 0); FLGMIN(); XD1(5, 6); SB0();
  MM(12, 1, 1, 1); XD1(5, 7); SB0();
  CHKFLAG(8); SB0();
  MM(13, 0, 0, 0); LDB(14, 0); WD1(10, 0); SB0();
  MM(13, 0, 0, 1); LDA(14, 0, 0); VMWAIT(11); PUB(9); SB0();
  MM(13, 0, 1, 0); LDB(14, 1); WD1(10, 1); SB0();
  MM(13, 0, 1, 1); LDA(14, 0, 1); SB0();
  MM(13, 1, 0, 0); LDA(14, 1, 0); SB0();
  MM(13, 1, 0, 1); LDA(14, 1, 1); SB0();
  MM(13, 1, 1, 0);  SB0();
  MM(13, 1, 1, 1);  SB0();
  MM(14, 0, 0, 0); LDB(15, 0); SB0();
  MM(14, 0, 0, 1); LDA(15, 0, 0); SB0();
  MM(14, 0, 1, 0); LDB(15, 1); RDFLAG(); SB0();
  MM(14, 0, 1, 1); LDA(15, 0, 1); SB0();
  MM(14, 1, 0, 0); LDA(15, 1, 0); SB0();
  MM(14, 1, 0, 1); LDA(15, 1, 1); SB0();
  MM(14, 1, 1, 0); FLGMIN(); SB0();
  MM(14, 1, 1, 1);  SB0();
  CHKFLAG(9); SB0();
  MM(15, 0, 0, 0); LDB(16, 0); WD1(11, 0); SB0();
  MM(15, 0, 0, 1); LDA(16, 0, 0); VMWAIT(11); PUB(10); SB0();
  MM(15, 0, 1, 0); LDB(16, 1); WD1(11, 1); SB0();
  MM(15, 0, 1, 1); LDA(16, 0, 1); SB0();
  MM(15, 1, 0, 0); LDA(16, 1, 0); XD1(6, 0); SB0();
  MM(15, 1, 0, 1); LDA(16, 1, 1); SB0();
  MM(15, 1, 1, 0); XD1(6, 1); SB0();
  MM(15, 1, 1, 1); XD1(6, 2); SB0();
  MM(16, 0, 0, 0); LDB(17, 0); XD1(6, 3); SB0();
  MM(16, 0, 0, 1); LDA(17, 0, 0); SB0();
  MM(16, 0, 1, 0); LDB(17, 1); RDFLAG(); XD1(6, 4); SB0();
  MM(16, 0, 1, 1); LDA(17, 0, 1); SB0();
  MM(16, 1, 0, 0); LDA(17, 1, 0); XD1(6, 5); SB0();
  MM(16, 1, 0, 1); LDA(17, 1, 1); SB0();
  MM(16, 1, 1, 0); FLGMIN(); XD1(6, 6); SB0();
  MM(16, 1, 1, 1); XD1(6, 7); SB0();
  CHKFLAG(10); SB0();
  MM(17, 0, 0, 0); LDB(18, 0); WD1(12, 0); SB0();
  MM(17, 0, 0, 1); LDA(18, 0, 0); VMWAIT(11); PUB(11); SB0();
  MM(17, 0, 1, 0); LDB(18, 1); WD1(12, 1); SB0();
  MM(17, 0, 1, 1); LDA(18, 0, 1); SB0();
  MM(17, 1, 0, 0); LDA(18, 1, 0); SB0();
  MM(17, 1, 0, 1); LDA(18, 1, 1); SB0();
  MM(17, 1, 1, 0);  SB0();
  MM(17, 1, 1, 1);  SB0();
  MM(18, 0, 0, 0); LDB(19, 0); SB0();
  MM(18, 0, 0, 1); LDA(19, 0, 0); SB0();
  MM(18, 0, 1, 0); LDB(19, 1); RDFLAG(); SB0();
  MM(18, 0, 1, 1); LDA(19, 0, 1); SB0();
  MM(18, 1, 0, 0); LDA(19, 1, 0); SB0();
  MM(18, 1, 0, 1); LDA(19, 1, 1); SB0();
  MM(18, 1, 1, 0); FLGMIN(); SB0();
  MM(18, 1, 1, 1);  SB0();
  CHKFLAG(11); SB0();
  MM(19, 0, 0, 0); LDB(20, 0); WD1(13, 0); SB0();
  MM(19, 0, 0, 1); LDA(20, 0, 0); VMWAIT(11); PUB(12); SB0();
  MM(19, 0, 1, 0); LDB(20, 1); WD1(13, 1); SB0();
  MM(19, 0, 1, 1); LDA(20, 0, 1); SB0();
  MM(19, 1, 0, 0); LDA(20, 1, 0); SB0();
  MM(19, 1, 0, 1); LDA(20, 1, 1); SB0();
  MM(19, 1, 1, 0);  SB0();
  MM(19, 1, 1, 1);  SB0();
  MM(20, 0, 0, 0); LDB(21, 0); SB0();
  MM(20, 0, 0, 1); LDA(21, 0, 0); SB0();
  MM(20, 0, 1, 0); LDB(21, 1); RDFLAG(); SB0();
  MM(20, 0, 1, 1); LDA(21, 0, 1); SB0();
  MM(20, 1, 0, 0); LDA(21, 1, 0); SB0();
  MM(20, 1, 0, 1); LDA(21, 1, 1); SB0();
  MM(20, 1, 1, 0); FLGMIN(); SB0();
  MM(20, 1, 1, 1);  SB0();
  CHKFLAG(12); SB0();
  MM(21, 0, 0, 0); LDB(22, 0); SB0();
  MM(21, 0, 0, 1); LDA(22, 0, 0); VMWAIT(2); PUB(13); SB0();
  MM(21, 0, 1, 0); LDB(22, 1); SB0();
  MM(21, 0, 1, 1); LDA(22, 0, 1); SB0();
  MM(21, 1, 0, 0); LDA(22, 1, 0); SB0();
  MM(21, 1, 0, 1); LDA(22, 1, 1); SB0();
  MM(21, 1, 1, 0);  SB0();
  MM(21, 1, 1, 1);  SB0();
  MM(22, 0, 0, 0); LDB(23, 0); SB0();
  MM(22, 0, 0, 1); LDA(23, 0, 0); SB0();
  MM(22, 0, 1, 0); LDB(23, 1); RDFLAG(); SB0();
  MM(22, 0, 1, 1); LDA(23, 0, 1); SB0();
  MM(22, 1, 0, 0); LDA(23, 1, 0); SB0();
  MM(22, 1, 0, 1); LDA(23, 1, 1); SB0();
  MM(22, 1, 1, 0); FLGMIN(); SB0();
  MM(22, 1, 1, 1);  SB0();
  CHKFLAG(13); SB0();
  MM(23, 0, 0, 0); LDB(24, 0); SB0();
  MM(23, 0, 0, 1); LDA(24, 0, 0); VMWAIT(0); PUB(14); SB0();
  MM(23, 0, 1, 0); LDB(24, 1); SB0();
  MM(23, 0, 1, 1); LDA(24, 0, 1); SB0();
  MM(23, 1, 0, 0); LDA(24, 1, 0); SB0();
  MM(23, 1, 0, 1); LDA(24, 1, 1); SB0();
  MM(23, 1, 1, 0);  SB0();
  MM(23, 1, 1, 1);  SB0();
  MM(24, 0, 0, 0); LDB(25, 0); SB0();
  MM(24, 0, 0, 1); LDA(25, 0, 0); SB0();
  MM(24, 0, 1, 0); LDB(25, 1); RDFLAG(); SB0();
  MM(24, 0, 1, 1); LDA(25, 0, 1); SB0();
  MM(24, 1, 0, 0); LDA(25, 1, 0); SB0();
  MM(24, 1, 0, 1); LDA(25, 1, 1); SB0();
  MM(24, 1, 1, 0); FLGMIN(); SB0();
  MM(24, 1, 1, 1); SB0();
  CHKFLAG(14); SB0();
  MM(25, 0, 0, 0); LDB(26, 0); SB0();
  MM(25, 0, 0, 1); LDA(26, 0, 0); SB0();
  MM(25, 0, 1, 0); LDB(26, 1); SB0();
  MM(25, 0, 1, 1); LDA(26, 0, 1); SB0();
  MM(25, 1, 0, 0); LDA(26, 1, 0); SB0();
  MM(25, 1, 0, 1); LDA(26, 1, 1); SB0();
  MM(25, 1, 1, 0); SB0();
  MM(25, 1, 1, 1);  SB0();
  MM(26, 0, 0, 0); LDB(27, 0); SB0();
  MM(26, 0, 0, 1); LDA(27, 0, 0); SB0();
  MM(26, 0, 1, 0); LDB(27, 1); SB0();
  MM(26, 0, 1, 1); LDA(27, 0, 1); SB0();
  MM(26, 1, 0, 0); LDA(27, 1, 0); SB0();
  MM(26, 1, 0, 1); LDA(27, 1, 1); SB0();
  MM(26, 1, 1, 0); SB0();
  MM(26, 1, 1, 1);  SB0();
  MM(27, 0, 0, 0); SB0();
  MM(27, 0, 0, 1); SB0();
  MM(27, 0, 1, 0); SB0();
  MM(27, 0, 1, 1);  SB0();
  MM(27, 1, 0, 0); SB0();
  MM(27, 1, 0, 1);  SB0();
  MM(27, 1, 1, 0);  SB0();
  MM(27, 1, 1, 1);  SB0();
#undef XD1
#undef WD1
#undef VMWAIT
#undef PUB
#undef RDFLAG
#undef FLGMIN
#undef CHKFLAG
#undef WOFF
#undef RC
#undef PRO_BARRIER
#undef LDA
#undef LDB
#undef MM
}

template <int G> DEV void ffn1_epi_part(rsrc_t rs, int vo, f32x16 (&acc)[2][2][2]) {
  constexpr int ph = G >> 2, Z = (G >> 1) & 1, u = G & 1;
  u32x4 hv;
#pragma unroll
  for (int p = 0; p < 4; ++p) {
    const int i = 8 * u + 2 * p;
    hv[p] = pk_bf16(gelu_mul(acc[0][ph][Z][i], acc[1][ph][Z][i]), gelu_mul(acc[0][ph][Z][i + 1], acc[1][ph][Z][i + 1]));
  }
  st128s(rs, vo + (2 * ph + u) * 1024 + Z * 112 * 1024, hv);
  SB0();
}

DEV void prep_wd_issue(u32x4 (&rw)[8], const u16* __restrict__ wd, int g, int lane) {
  const int seg = lane < 56 ? lane : 55;
  const u16* src = wd + (size_t)(16 * (g >> 1) + 4 * (g & 1)) * D_ + seg * 8;
#pragma unroll
  for (int j = 0; j < 8; ++j) rw[j] = *(const u32x4*)(src + (size_t)((j & 3) + 8 * (j >> 2)) * D_);
}
DEV void prep_wd_finish(const u32x4 (&rw)[8], rsrc_t rs, int g, LAS char* stg, int lane) {
  const int seg = lane < 56 ? lane : 55;
  const int m1 = seg >> 3, j = seg & 7;
  const int rho0 = 16 * ((m1 >> 1) & 1) + 4 * (m1 & 1);
#pragma unroll
  for (int e2 = 0; e2 < 4; ++e2) {
    u32x4 o0, o1;
#pragma unroll
    for (int p = 0; p < 4; ++p) {
      o0[p] = perm_lo(rw[2 * p + 1][e2], rw[2 * p][e2]);
      o1[p] = perm_hi(rw[2 * p + 1][e2], rw[2 * p][e2]);
    }
    const int rho = rho0 + 8 * (e2 >> 1) + 2 * (e2 & 1);
    const int pos = (m1 < 4) ? rho : (32 + (rho < 20 ? rho : rho - 4));
    *(LAS u32x4*)(stg + j * 912 + pos * 16) = o0;
    *(LAS u32x4*)(stg + j * 912 + pos * 16 + 16) = o1;
  }
  WFENCE();
  const int hh = g & 1;
  const int pos = lane < 32 ? hh * 32 + lane : 64 + hh * 24 + (lane - 32);
  const int vo = lane < 56 ? WS_WD + (g >> 1) * 1792 + pos * 16 : (int)0x7ff00000;
#pragma unroll
  for (int jj = 0; jj < 8; ++jj) {
    u32x4 v = *(LAS u32x4*)(stg + jj * 912 + seg * 16);
    st128s(rs, lane < 56 ? vo + jj * 112 * 1792 : vo, v);
  }
  WFENCE();
}
DEV void prep_gate(u32x4 gv, rsrc_t rs, int a, int w, LAS char* xt, LAS char* sc, int lane) {
  const int lc = lane < 56 ? lane : 0;
  float gwf[8];
#pragma unroll
  for (int p = 0; p < 4; ++p) { gwf[2 * p] = lane < 56 ? bflo(gv[p]) : 0.f; gwf[2 * p + 1] = lane < 56 ? bfhi(gv[p]) : 0.f; }
#pragma unroll
  for (int i = 0; i < 8; ++i) {
    u32x4 xv = *(LAS u32x4*)(xt + (8 * w + i) * AXS + lc * 16);
    float sacc = 0.f;
#pragma unroll
    for (int p = 0; p < 4; ++p) {
      u32 pr = pk_bf16(bflo(xv[p]) * gwf[2 * p], bfhi(xv[p]) * gwf[2 * p + 1]);
      sacc += bflo(pr);
      sacc += bfhi(pr);
    }
    *(LAS float*)(sc + i * 272 + lane * 4) = sacc;
  }
  WFENCE();
  f32x4 v0 = *(LAS f32x4*)(sc + (lane & 7) * 272 + (lane >> 3) * 32), v1 = *(LAS f32x4*)(sc + (lane & 7) * 272 + (lane >> 3) * 32 + 16);
  float tot = ((v0[0] + v0[1]) + (v0[2] + v0[3])) + ((v1[0] + v1[1]) + (v1[2] + v1[3]));
  tot += __builtin_bit_cast(float, __builtin_amdgcn_ds_bpermute((lane ^ 8) << 2, __builtin_bit_cast(int, tot)));
  tot += __builtin_bit_cast(float, __builtin_amdgcn_ds_bpermute((lane ^ 16) << 2, __builtin_bit_cast(int, tot)));
  tot += __builtin_bit_cast(float, __builtin_amdgcn_ds_bpermute((lane ^ 32) << 2, __builtin_bit_cast(int, tot)));
  if (lane < 8) {
    float z = rbf(tot);
    float g = __builtin_amdgcn_rcpf(1.0f + __builtin_amdgcn_exp2f(-1.4426950408889634f * z));
    st32s(rs, WS_GATE + (a * 64 + 8 * w + lane) * 4, __builtin_bit_cast(u32, rbf(g)));
  }
  WFENCE();
}

DEV cf ldtw2048(int i) { return cf{gTw2048.v[i][0], gTw2048.v[i][1]}; }
DEV cf ldtw448(int i) { return cf{gTw448.v[i][0], gTw448.v[i][1]}; }
DEV void lst(LAS char* p, cf v) { *(LAS f32x2*)p = f32x2{v.x, v.y}; }
DEV cf lld(LAS char* p) { f32x2 t = *(LAS f32x2*)p; return cf{t[0], t[1]}; }

struct FftPre { u32x4 wf[2]; u32 wx[4]; f32x4 tw[4]; cf u1[8], u2[8], u3[8]; };
DEV void fft_prefetch_static(FftPre& f, const u16* __restrict__ w_r, const u16* __restrict__ w_i, int item, int w, int lane) {
  const int k1 = item / 7, l1 = item % 7;
  const int row = lane >> 3;
  f.wx[0] = f.wx[1] = f.wx[2] = f.wx[3] = 0u;
  if (l1 == 0) {
    const int k2 = 2 * w + (row >> 2) + 8 * (row & 3);
    const int nks = (S_ - (k1 + 64 * k2)) & (S_ - 1);
    f.wx[0] = w_r[nks * DF_]; f.wx[1] = w_i[nks * DF_]; f.wx[2] = w_r[nks * DF_ + 224]; f.wx[3] = w_i[nks * DF_ + 224];
  }
#pragma unroll
  for (int i = 0; i < 4; ++i) f.tw[i] = *(const f32x4*)&gTwb.v[l1][i][lane & 7][0];
#pragma unroll
  for (int i = 0; i < 8; ++i) {
    f.u1[i] = ldtw2048(4 * i * k1); f.u2[i] = ldtw2048(w * (k1 + 64 * i)); f.u3[i] = ldtw448(8 * i * l1);
    asm volatile("" : "+s"(f.u2[i].x), "+s"(f.u2[i].y));
    if (i) asm volatile("" : "+s"(f.u1[i].x), "+s"(f.u1[i].y), "+s"(f.u3[i].x), "+s"(f.u3[i].y));
  }
}
DEV void fft_load_filter(FftPre& f, rsrc_t rs, int item, int w, int lane) {
  f.wf[0] = ld128s(rs, WS_WF + item * 8192 + w * 2048 + lane * 16);
  f.wf[1] = ld128s(rs, WS_WF + item * 8192 + w * 2048 + 1024 + lane * 16);
}
#define TWB(al) (cf{f.tw[(al) >> 1][2 * ((al) & 1)], f.tw[(al) >> 1][2 * ((al) & 1) + 1]})

constexpr int FRS = 960 + 16;
template <bool ISSUE, bool FINISH>
DEV void prep_filter(const u16* __restrict__ w_r, const u16* __restrict__ w_i, rsrc_t rs, int hu, LAS char* st, int lane, u32& a224, u32& b224) {
  const int k1 = hu >> 3, tb = (hu >> 1) & 3, hb = hu & 1;
  const int row = lane >> 3, lo3 = lane & 7;
  const int k2 = 2 * tb + (row >> 2) + 8 * (row & 3);
  const int ks = k1 + 64 * k2, nks = (S_ - ks) & (S_ - 1);
  rsrc_t rr = __builtin_amdgcn_make_buffer_rsrc((void*)w_r, 0, S_ * DF_ * 2, 0x00020000);
  rsrc_t ri = __builtin_amdgcn_make_buffer_rsrc((void*)w_i, 0, S_ * DF_ * 2, 0x00020000);
  const u32 lb = (u32)(size_t)st;
  const int second = lane >= 30 ? 1 : 0;
#define FD(arr, i) do { const int rw_ = 2 * (i) + second; const int kk = k1 + 64 * (2 * tb + (rw_ >> 2) + 8 * (rw_ & 3)); \
      const int r = hb ? ((S_ - kk) & (S_ - 1)) : kk; \
      bdma<((arr) * 4 + (i)) * FRS, 0>((arr) ? ri : rr, (u32)(((r * (DF_ * 2)) & ~15) + (lane - 30 * second) * 16), lb); } while (0)
  if (ISSUE && lane < 60) { FD(0, 0); FD(0, 1); FD(0, 2); FD(0, 3); FD(1, 0); FD(1, 1); FD(1, 2); FD(1, 3); }
#undef FD
  if (ISSUE) { a224 = w_r[ks * DF_ + 224]; b224 = w_i[ks * DF_ + 224]; }
  if (!FINISH) return;
  if (ISSUE) DRAIN();
  const u32 w224 = a224 | (b224 << 16);
  constexpr int IMO = 4 * FRS;
  const int ab = (row >> 1) * FRS + (row & 1) * 480 +
                 (hb ? ((nks * (DF_ * 2)) & 15) + (D_ - 6 - 7 * lo3) * 2 - 4 * 112 : ((ks * (DF_ * 2)) & 15) + lo3 * 14);
  const u32 sh = (ab & 2) * 8;
  LAS char* pb = st + (ab & ~3);
  const int stp = hb ? -112 : 112;
  const u32 sgn = hb ? 0x80000000u : 0u;
  u32 o[7][4];
#pragma unroll
  for (int b4 = 0; b4 < 4; ++b4) {
    LAS char* qp = pb + b4 * stp;
    u32 r[5], m[5], er[4], em[4];
#pragma unroll
    for (int i = 0; i < 4; ++i) { r[i] = *(LAS u32*)(qp + 4 * i); m[i] = *(LAS u32*)(qp + 4 * i + IMO); }
    r[4] = 0u; m[4] = 0u;
#pragma unroll
    for (int i = 0; i < 4; ++i) { er[i] = __builtin_amdgcn_alignbit(r[i + 1], r[i], sh); em[i] = __builtin_amdgcn_alignbit(m[i + 1], m[i], sh); }
#pragma unroll
    for (int e = 0; e < 7; ++e) {
      u32 v = ((e & 1) ? perm_hi(em[e >> 1], er[e >> 1]) : perm_lo(em[e >> 1], er[e >> 1])) ^ sgn;
      if (b4 == 0 && e == 6) v = (hb && lo3 == 0) ? w224 : v;
      o[e][b4] = v;
    }
  }
  const int ob = WS_WF + (k1 * 7 + (hb ? 6 : 0)) * 8192 + tb * 2048 + hb * 1024 + lane * 16;
  const int os = hb ? -8192 : 8192;
#pragma unroll
  for (int e = 0; e < 7; ++e) st128s(rs, ob + e * os, u32x4{o[e][0], o[e][1], o[e][2], o[e][3]});
}
DEV void aux_all(const u16* __restrict__ x, const u16* __restrict__ w_r, const u16* __restrict__ w_i, const u16* __restrict__ gw, LAS char* lds3, rsrc_t rs, int a, int w, int lane) {
  LAS char* fst = lds3 + L_AG + w * 7808;
  const bool xf = w < 2 && a < 20;
  u32 a224 = 0, b224 = 0;
  u32x4 gv;
  const int kh = w >> 2, mq = w & 3, q = lane >> 4;
  bf16x8 af[4][2];
  u32x2 ep;
  {
    const int tid = w * 64 + lane, r0 = tid >> 3, pc = tid & 7;
    const u16* xp = x + (size_t)(32 * r0 + a) * D_ + pc * 8;
    u32x4 t[7];
#pragma unroll
    for (int c = 0; c < 7; ++c) t[c] = *(const u32x4*)(xp + c * 64);
    SB0();
#pragma unroll
    for (int i = 0; i < 4; ++i)
#pragma unroll
      for (int hl = 0; hl < 2; ++hl) af[i][hl] = *(const bf16x8*)&gT1.v[kh][i][hl][lane][0];
    ep = ld64s(rs, w == 7 ? OFF_E : (int)0x7ff00000);
    SB0();
    gv = *(const u32x4*)(gw + (lane < 56 ? lane : 0) * 8);
    SB0();
    wg_barrier();
    LAS char* d = lds3 + L_X + r0 * AXS + pc * 16;
#pragma unroll
    for (int c = 0; c < 7; ++c) *(LAS u32x4*)(d + c * 128) = t[c];
  }
  lds_set(lds3, C_G + w, 1u, lane);
  put_tag(lds3, ep, w, lane);
  asm volatile("" : "+v"(af[0][0]), "+v"(af[0][1]), "+v"(af[1][0]), "+v"(af[1][1]), "+v"(af[2][0]), "+v"(af[2][1]), "+v"(af[3][0]), "+v"(af[3][1]));
  if (xf) prep_filter<true, false>(w_r, w_i, rs, 224 + 2 * a + w, fst, lane, a224, b224);
  SB0();
  lds_wait8(lds3, C_G, 1u);
  f32x4 acc[2][7];
  {
    const int qq = (lane & 15) >> 2, pp = lane & 3;
    LAS char* tbp = lds3 + L_X + (8 * q + qq) * AXS + (16 * mq + 4 * pp) * 2;
    bf16x8 b[2][7];
#pragma unroll
    for (int ks = 0; ks < 2; ++ks)
#pragma unroll
      for (int m1 = 0; m1 < 7; ++m1) {
        LAS char* p = tbp + 32 * ks * AXS + m1 * 128;
        b[ks][m1] = __builtin_shufflevector(__builtin_amdgcn_ds_read_tr16_b64_v4i16((LAS s16x4*)(p)),
                                            __builtin_amdgcn_ds_read_tr16_b64_v4i16((LAS s16x4*)(p + 4 * AXS)), 0, 1, 2, 3, 4, 5, 6, 7);
      }
#pragma unroll
    for (int kt = 0; kt < 2; ++kt) {
#pragma unroll
      for (int m1 = 0; m1 < 7; ++m1) acc[kt][m1] = mfma16(af[2 * kt][1], b[0][m1], f32x4{0.f, 0.f, 0.f, 0.f});
#pragma unroll
      for (int m1 = 0; m1 < 7; ++m1) acc[kt][m1] = mfma16(af[2 * kt + 1][1], b[1][m1], acc[kt][m1]);
#pragma unroll
      for (int m1 = 0; m1 < 7; ++m1) acc[kt][m1] = mfma16(af[2 * kt][0], b[0][m1], acc[kt][m1]);
#pragma unroll
      for (int m1 = 0; m1 < 7; ++m1) acc[kt][m1] = mfma16(af[2 * kt + 1][0], b[1][m1], acc[kt][m1]);
    }
  }
  constexpr int IS = 2048 * 16;
  const int ob = WS_P + (8 * kh + q) * 7 * IS + (a * 64 + 16 * mq + (lane & 15)) * 16;
#pragma unroll
  for (int kt = 0; kt < 2; ++kt) {
    cf y[2][7];
    cf e32[7];
#pragma unroll
    for (int o = 0; o < 2; ++o) {
      cf z[7];
#pragma unroll
      for (int m1 = 0; m1 < 7; ++m1) z[m1] = {acc[kt][m1][2 * o], acc[kt][m1][2 * o + 1]};
      cf X0, A[3], B[3];
      dft7_ab(z, X0, A, B);
      const bool sp = (kt == 0) && (o == 0) && (kh == 0) && (q == 0);
      y[o][0] = {X0.x, sp ? 0.f : X0.y};
#pragma unroll
      for (int l = 0; l < 3; ++l) {
        y[o][l + 1] = {sp ? A[l].x : A[l].x + B[l].y, sp ? -B[l].x : A[l].y - B[l].x};
        y[o][6 - l] = {sp ? A[l].x : A[l].x - B[l].y, sp ? B[l].x : A[l].y + B[l].x};
      }
      if (kt == 0 && o == 0) {
        e32[0] = {X0.y, 0.f};
#pragma unroll
        for (int l = 0; l < 3; ++l) { e32[l + 1] = {A[l].y, -B[l].y}; e32[6 - l] = {A[l].y, B[l].y}; }
      }
    }
#pragma unroll
    for (int l = 0; l < 7; ++l)
      st128s(rs, ob + (4 * kt * 7 + l) * IS, __builtin_bit_cast(u32x4, f32x4{y[0][l].x, y[0][l].y, y[1][l].x, y[1][l].y}));
    if (kt == 0 && kh == 0 && q == 0) {
#pragma unroll
      for (int l = 0; l < 7; ++l)
        st128s(rs, ob + (16 * 7 + l) * IS, __builtin_bit_cast(u32x4, f32x4{e32[l].x, e32[l].y, 0.f, 0.f}));
    }
  }
  if (a == 0 && w == 0 && lane < 17) st32s(rs, OFF_CNT + lane * 128, 0u);
  DRAIN();
  if (lds_arrive(lds3, C_PCNT, lane) == 7u) {
    u32x2 tag = get_tag(lds3);
    if (lane < 8) st64s(rs, OFF_PF + lane * 256 + a * 8, tag);
  }
  if (w >= 4) return;
  if (xf) { prep_filter<false, true>(w_r, w_i, rs, 224 + 2 * a + w, fst, lane, a224, b224); WFENCE(); DRAIN(); }
  if (lds_arrive(lds3, C_WCNT, lane) == 3u) {
    u32x2 tag = get_tag(lds3);
    if (lane < 8) st64s(rs, OFF_QF + lane * 2048 + (224 + a) * 8, tag);
  }
  prep_gate(gv, rs, a, w, lds3 + L_X, fst, lane);
  prep_gate(gv, rs, a, w + 4, lds3 + L_X, fst, lane);
  DRAIN();
  if (lds_arrive(lds3, C_GCNT, lane) == 3u) {
    u32x2 tag = get_tag(lds3);
    if (lane == 0) st64s(rs, OFF_GF + a * 128, tag);
  }
}

DEV void fft_loadP(cf (&v)[8], rsrc_t rs, int item, int w, int lane) {
  const int k1 = item / 7, l1 = item % 7;
#pragma unroll
  for (int c = 0; c < 8; ++c) v[c] = ldc(rs, WS_P + ((((k1 >> 1) * 7 + l1) * 32 + 4 * c + w) * 64 + lane) * 16 + (k1 & 1) * 8);
}

DEV void fft_item(rsrc_t rs, FftPre& f, cf (&v)[8], int item, LAS char* lds3, int w, int lane, u32x4 qa, u32x4 qb, u32x2 tag, int qoff) {
  const int k1 = item / 7, l1 = item % 7;
  LAS char* base = lds3 + L_FA;
  LAS char* mine = base + w * 4608;
  const int row = lane >> 3, lo3 = lane & 7;
#pragma unroll
  for (int c = 1; c < 8; ++c) v[c] = cmul(v[c], f.u1[c]);
  dft8<0>(v);
#pragma unroll
  for (int g = 0; g < 8; ++g) v[g] = cmul(v[g], f.u2[g]);
  lds_wait1(lds3, C_QCNT, 4u);
#pragma unroll
  for (int g = 0; g < 8; ++g) lst(base + (g >> 1) * 4608 + (((g & 1) * 4 + w) * 64 + lane) * 8, v[g]);
  lds_set(lds3, C_FFT + w, 1u, lane);
  if (__builtin_amdgcn_ballot_w64(!(tag_ok2(qa, tag) && tag_ok2(qb, tag))) != 0ull) poll256(rs, qoff, tag, lane);
  __builtin_amdgcn_fence(__ATOMIC_ACQUIRE, "wavefront");
  fft_load_filter(f, rs, item, w, lane);
  if (w == 0) lds_set(lds3, C_QGO, 1u, lane);
  (void)lds_arrive(lds3, C_FIN, lane);
  SB0();
  lds_wait4(lds3, C_FFT, 1u);
#pragma unroll
  for (int i = 0; i < 8; ++i) v[i] = lld(mine + (i * 64 + lane) * 8);
  dft4<0>(v[0], v[1], v[2], v[3]);
  dft4<0>(v[4], v[5], v[6], v[7]);
  WFENCE();
#pragma unroll
  for (int i = 0; i < 8; ++i) lst(mine + i * 576 + lane * 8, v[i]);
  WFENCE();
#pragma unroll
  for (int a = 0; a < 8; ++a) v[a] = lld(mine + row * 576 + (8 * a + lo3) * 8);
#pragma unroll
  for (int a = 1; a < 8; ++a) v[a] = cmul(v[a], f.u3[a]);
  dft8<0>(v);
#pragma unroll
  for (int al = 0; al < 8; ++al) v[al] = cmul(v[al], TWB(al));
  WFENCE();
#pragma unroll
  for (int al = 0; al < 8; ++al) lst(mine + row * 576 + al * 72 + lo3 * 8, v[al]);
  WFENCE();
#pragma unroll
  for (int bb = 0; bb < 8; ++bb) v[bb] = lld(mine + row * 576 + lo3 * 72 + bb * 8);
  dft8<0>(v);
#pragma unroll
  for (int be = 0; be < 8; ++be) {
    const int kd = l1 + 7 * lo3 + 56 * be;
    cf wf = {bflo(f.wf[be >> 2][be & 3]), bfhi(f.wf[be >> 2][be & 3])};
    if (be == 0 && kd == 0) wf = {0.5f * (wf.x + bflo(f.wx[0])), 0.5f * (wf.y - bflo(f.wx[1]))};
    if (be == 4 && kd == 224) wf = {0.5f * (wf.x + bflo(f.wx[2])), 0.5f * (wf.y - bflo(f.wx[3]))};
    v[be] = cmul(v[be], wf);
  }
  dft8<1>(v);
  WFENCE();
#pragma unroll
  for (int bb = 0; bb < 8; ++bb) lst(mine + row * 576 + lo3 * 72 + bb * 8, v[bb]);
  WFENCE();
#pragma unroll
  for (int al = 0; al < 8; ++al) v[al] = lld(mine + row * 576 + al * 72 + lo3 * 8);
#pragma unroll
  for (int al = 0; al < 8; ++al) v[al] = cmulc(v[al], TWB(al));
  dft8<1>(v);
#pragma unroll
  for (int a = 1; a < 8; ++a) v[a] = cmulc(v[a], f.u3[a]);
  WFENCE();
#pragma unroll
  for (int a = 0; a < 8; ++a) lst(mine + row * 576 + (8 * a + lo3) * 8, v[a]);
  WFENCE();
#pragma unroll
  for (int i = 0; i < 8; ++i) v[i] = lld(mine + i * 576 + lane * 8);
  dft4<1>(v[0], v[1], v[2], v[3]);
  dft4<1>(v[4], v[5], v[6], v[7]);
  WFENCE();
#pragma unroll
  for (int i = 0; i < 8; ++i) lst(mine + (i * 64 + lane) * 8, v[i]);
  lds_set(lds3, C_FFT + w, 2u, lane);
  lds_wait4(lds3, C_FFT, 2u);
#pragma unroll
  for (int g = 0; g < 8; ++g) v[g] = lld(base + (g >> 1) * 4608 + (((g & 1) * 4 + w) * 64 + lane) * 8);
#pragma unroll
  for (int g = 0; g < 8; ++g) v[g] = cmulc(v[g], f.u2[g]);
  dft8<1>(v);
#pragma unroll
  for (int c = 1; c < 8; ++c) v[c] = cmulc(v[c], f.u1[c]);
#pragma unroll
  for (int c = 0; c < 8; ++c) stc(rs, WS_R + ((item * 32 + 4 * c + w) * 64 + lane) * 8, v[c].x, v[c].y);
}

DEV void final_phase(rsrc_t rs, u16* __restrict__ out, int n2, int j, LAS char* lds3, int w, int lane, const bf16x8 (&bt)[4][2], float g) {
  const int tid = w * 64 + lane;
  const int t = w >> 1, st = w & 1, r = lane & 31, hh = lane >> 5;
  const int k1 = tid >> 3, e = tid & 7;
  const int ro = WS_R + ((k1 * 7 * 32 + n2) * 64 + 8 * j + e) * 8;
  cf z[7];
#pragma unroll
  for (int l = 0; l < 7; ++l) z[l] = ldc(rs, ro + l * 16384);
  if (k1 == 0) {
#pragma unroll
    for (int l = 0; l < 7; ++l) { cf v = ldc(rs, ro + (32 * 7 + l) * 16384); z[l] = {z[l].x - v.y, z[l].y + v.x}; }
  }
  const int n1 = 32 * st + r, s = 32 * n1 + n2;
  lds_wait1(lds3, C_RCNT, 4u);
  LAS char* at = lds3 + L_INV;
  {
    cf X0, A[3], B[3], y[7];
    dft7_ab(z, X0, A, B);
    y[0] = X0;
#pragma unroll
    for (int l = 0; l < 3; ++l) {
      y[l + 1] = {A[l].x - B[l].y, A[l].y + B[l].x};
      y[6 - l] = {A[l].x + B[l].y, A[l].y - B[l].x};
    }
#pragma unroll
    for (int m1 = 0; m1 < 7; ++m1) {
      const int rowi = 32 * (m1 >> 2) + 16 * ((m1 >> 1) & 1) + 8 * (e >> 2) + 4 * (m1 & 1) + (e & 3);
      u32 hi = pk_bf16(y[m1].x, y[m1].y);
      u32 lo = pk_bf16(y[m1].x - bflo(hi), y[m1].y - bfhi(hi));
      *(LAS u32*)(at + rowi * 144 + k1 * 4) = hi;
      *(LAS u32*)(at + 64 * 144 + rowi * 144 + k1 * 4) = lo;
    }
  }
  lds_set(lds3, C_INV + w, 1u, lane);
  lds_wait4(lds3, C_INV, 1u);
  f32x16 acc;
#pragma unroll
  for (int i = 0; i < 16; ++i) acc[i] = 0.f;
#pragma unroll
  for (int ks = 0; ks < 4; ++ks) {
    LAS char* ap = at + (32 * t + r) * 144 + ks * 32 + hh * 16;
    bf16x8 ah = *(LAS bf16x8*)ap;
    bf16x8 al = *(LAS bf16x8*)(ap + 64 * 144);
    acc = mfma32(al, bt[ks][0], acc);
    acc = mfma32(ah, bt[ks][1], acc);
    acc = mfma32(ah, bt[ks][0], acc);
  }
  lds_wait4(lds3, C_B, 1u);
  f32x4 bq[4];
#pragma unroll
  for (int qd = 0; qd < 4; ++qd) {
    const LAS f32x4* bp = (const LAS f32x4*)(lds3 + L_X + (w >> 1) * 32768) + ((w & 1) * 4 + qd) * 64 + lane;
    bq[qd] = (bp[0] + bp[512]) + (bp[1024] + bp[1536]);
  }
  const float omg = rbf(1.0f - g);
#pragma unroll
  for (int u = 0; u < 2; ++u) {
    const int gseg = 4 * t + 2 * u + hh;
    if (gseg < 7) {
      u32x4 o;
#pragma unroll
      for (int v = 0; v < 2; ++v)
#pragma unroll
        for (int i2 = 0; i2 < 2; ++i2) {
          float r2[2];
#pragma unroll
          for (int k = 0; k < 2; ++k) {
            const int i = 2 * i2 + k;
            float a = rbf(acc[8 * u + 4 * v + i]);
            float b = rbf(bq[2 * u + v][i]);
            r2[k] = rbf(g * a) + rbf(omg * b);
          }
          o[2 * v + i2] = pk_bf16(r2[0], r2[1]);
        }
      *(u32x4*)(out + (size_t)s * D_ + 64 * gseg + 8 * j) = o;
    }
  }
}

NOPK __global__ __launch_bounds__(512, 2) __attribute__((amdgpu_waves_per_eu(2, 2)))
void mega_kernel(const u16* __restrict__ x, const u16* __restrict__ w_r, const u16* __restrict__ w_i,
                 const u16* __restrict__ wg, const u16* __restrict__ wu, const u16* __restrict__ wd,
                 const u16* __restrict__ gw, char* __restrict__ ws, u16* __restrict__ out) {
  extern __shared__ __attribute__((aligned(16))) char lds[];
  LAS char* lds3 = (LAS char*)lds;
  u64 pc_start;
  asm volatile("s_getpc_b64 %0" : "=s"(pc_start));
  asm volatile("" :: "s"(x), "s"(wg), "s"(wu), "s"(ws), "s"(wd), "s"(w_r), "s"(w_i), "s"(gw), "s"(out));
  const int tid = threadIdx.x, w = __builtin_amdgcn_readfirstlane(tid >> 6), lane = tid & 63;
  const int b = blockIdx.x, xcd = b & 7, idx = b >> 3;
  rsrc_t rs = __builtin_amdgcn_make_buffer_rsrc(ws, 0, WS_END, 0x00020000);
  const bool gemm = idx < 28;
  if (w == 7) *(LAS u32*)(lds3 + L_CTL + lane * 4) = 0u;
  if (gemm && w >= 4) { wg_barrier(); }
  const int rb = (xcd >> 2) * 4 + (idx & 3), fb = (xcd & 3) * 7 + (idx >> 2);
  const int n2 = xcd * 4 + (idx & 3), j = idx >> 2;
  const int aux_a = xcd * 4 + (idx - 28);
  const int wl = w & 3;
  if (__builtin_expect(!gemm, 0)) aux_all(x, w_r, w_i, gw, lds3, rs, aux_a, w, lane);
  if (__builtin_expect(w < 4, 1)) {
    const int rho = lane & 31, hh = lane >> 5;
    const int wbase = WS_WD + (j * 112 + wl) * 1792;
    const int wo0 = wbase + lane * 16;
    const int p1 = 1024 + (hh * 24 + (rho < 20 ? rho : (rho & 4) ? 0 : rho - 4)) * 16;
    const int wo1 = (rho & 20) == 20 ? (int)0x7ff00000 : wbase + p1;
    const int ho0 = WS_H + ((2 * n2) * 112 + wl) * 1024 + lane * 16;
    const int wo1x = lane < 48 ? wo0 : (int)0x7ff00000;
    u32 qrdy = 0;
    const int rot2 = ROT2 ? (j * 7) >> 1 : 0;
#define RS(s) ((s) + rot2 >= 28 ? (s) + rot2 - 28 : (s) + rot2)
    LAS char* xl_p = lds3 + L_X + wl * 8192;
    LAS char* wl_p = lds3 + L_W + wl * 10240;
    const u32 xl_u = (u32)(size_t)xl_p, wl_u = (u32)(size_t)wl_p;
    if (gemm) {
      f32x16 acc[2][2][2];
      __builtin_amdgcn_s_setprio(1);
      ffn1_core(x, wg, wu, 4 * rb + wl, fb, lds3, wl, lane, acc, rs, wo0, wo1x, qrdy, ROT1 ? (fb + 2 * (rb & 3)) % 7 : 0);
      const int hvo = WS_H + ((2 * (4 * rb + wl)) * 112 + 4 * fb) * 1024 + lane * 16;
      __builtin_amdgcn_s_setprio(EPRIO);
      ffn1_epi_part<0>(rs, hvo, acc); ffn1_epi_part<1>(rs, hvo, acc); ffn1_epi_part<7>(rs, hvo, acc);
      u32 qv = *(lds_vu32*)(lds3 + L_CTL + C_FIN * 4);
      ffn1_epi_part<2>(rs, hvo, acc);
      qrdy = (u32)__builtin_amdgcn_readfirstlane(qv) >= 4u;
      __builtin_amdgcn_fence(__ATOMIC_ACQUIRE, "workgroup");
      if (qrdy) { bdma_sc1b<0, 0>(rs, (u32)(wo0), xl_u, (u32)(RS(11) * 7168)); bdma_sc1b<2048, 0>(rs, (u32)(wo0), xl_u, (u32)(RS(12) * 7168)); bdma_sc1b<4096, 0>(rs, (u32)(wo0), xl_u, (u32)(RS(13) * 7168)); bdma_sc1b<1024, 1024>(rs, (u32)(wo1x), xl_u, (u32)(RS(11) * 7168)); bdma_sc1b<3072, 1024>(rs, (u32)(wo1x), xl_u, (u32)(RS(12) * 7168)); bdma_sc1b<5120, 1024>(rs, (u32)(wo1x), xl_u, (u32)(RS(13) * 7168)); }
      SB0();
      ffn1_epi_part<3>(rs, hvo, acc);
      if (qrdy) { bdma_sc1b<6144, 0>(rs, (u32)(wo0), xl_u, (u32)(RS(14) * 7168)); bdma_sc1b<32768, 0>(rs, (u32)(wo0), xl_u, (u32)(RS(15) * 7168)); bdma_sc1b<34816, 0>(rs, (u32)(wo0), xl_u, (u32)(RS(16) * 7168)); bdma_sc1b<7168, 1024>(rs, (u32)(wo1x), xl_u, (u32)(RS(14) * 7168)); bdma_sc1b<33792, 1024>(rs, (u32)(wo1x), xl_u, (u32)(RS(15) * 7168)); bdma_sc1b<35840, 1024>(rs, (u32)(wo1x), xl_u, (u32)(RS(16) * 7168)); }
      SB0();
      ffn1_epi_part<4>(rs, hvo, acc);
      if (qrdy) { bdma_sc1b<36864, 0>(rs, (u32)(wo0), xl_u, (u32)(RS(17) * 7168)); bdma_sc1b<38912, 0>(rs, (u32)(wo0), xl_u, (u32)(RS(18) * 7168)); bdma_sc1b<65536, 0>(rs, (u32)(wo0), xl_u, (u32)(RS(19) * 7168)); bdma_sc1b<37888, 1024>(rs, (u32)(wo1x), xl_u, (u32)(RS(17) * 7168)); bdma_sc1b<39936, 1024>(rs, (u32)(wo1x), xl_u, (u32)(RS(18) * 7168)); bdma_sc1b<66560, 1024>(rs, (u32)(wo1x), xl_u, (u32)(RS(19) * 7168)); }
      SB0();
      ffn1_epi_part<5>(rs, hvo, acc);
      if (qrdy) { bdma_sc1b<67584, 0>(rs, (u32)(wo0), xl_u, (u32)(RS(20) * 7168)); bdma_sc1b<69632, 0>(rs, (u32)(wo0), xl_u, (u32)(RS(21) * 7168)); bdma_sc1b<71680, 0>(rs, (u32)(wo0), xl_u, (u32)(RS(22) * 7168)); bdma_sc1b<68608, 1024>(rs, (u32)(wo1x), xl_u, (u32)(RS(20) * 7168)); bdma_sc1b<70656, 1024>(rs, (u32)(wo1x), xl_u, (u32)(RS(21) * 7168)); bdma_sc1b<72704, 1024>(rs, (u32)(wo1x), xl_u, (u32)(RS(22) * 7168)); }
      SB0();
      ffn1_epi_part<6>(rs, hvo, acc);
      __builtin_amdgcn_s_setprio(0);
    }
    const u32x2 tag = get_tag(lds3);
    DRAIN();
    lds_wait1(lds3, C_PGO, 1u);
    if (lds_arrive(lds3, C_ADONE, lane) == 3u) {
      if (gemm && glb_arrive(ws, rb, lane) == 27u) { if (lane == 0) st64s(rs, OFF_HB + rb * 128, tag); }
    }
    lds_wait1(lds3, C_FIN, 4u);
    lds_wait1(lds3, C_WGO, 1u);
    u32x4 wdv[11][2], hb[28][2];
#pragma unroll
    for (int s = 0; s < 11; ++s) {
      wdv[s][0] = __builtin_amdgcn_raw_buffer_load_b128(rs, wo0, RS(s) * 7168, AUX_SC1);
      wdv[s][1] = __builtin_amdgcn_raw_buffer_load_b128(rs, wo1, RS(s) * 7168, AUX_SC1);
    }
    SB0();
    lds_wait1(lds3, C_ADONE, 4u);
    if (!gemm) { lds_wait1(lds3, C_GCNT, 4u); lds_wait1(lds3, C_PCNT, 8u); }
    bdma_sc1b<0, 0>(rs, (u32)(wo0), wl_u, (u32)(RS(23) * 7168)); bdma_sc1b<2048, 0>(rs, (u32)(wo0), wl_u, (u32)(RS(24) * 7168)); bdma_sc1b<4096, 0>(rs, (u32)(wo0), wl_u, (u32)(RS(25) * 7168)); bdma_sc1b<6144, 0>(rs, (u32)(wo0), wl_u, (u32)(RS(26) * 7168)); bdma_sc1b<8192, 0>(rs, (u32)(wo0), wl_u, (u32)(RS(27) * 7168)); bdma_sc1b<1024, 1024>(rs, (u32)(wo1x), wl_u, (u32)(RS(23) * 7168)); bdma_sc1b<3072, 1024>(rs, (u32)(wo1x), wl_u, (u32)(RS(24) * 7168)); bdma_sc1b<5120, 1024>(rs, (u32)(wo1x), wl_u, (u32)(RS(25) * 7168)); bdma_sc1b<7168, 1024>(rs, (u32)(wo1x), wl_u, (u32)(RS(26) * 7168)); bdma_sc1b<9216, 1024>(rs, (u32)(wo1x), wl_u, (u32)(RS(27) * 7168));
    if (!qrdy) { bdma_sc1b<0, 0>(rs, (u32)(wo0), xl_u, (u32)(RS(11) * 7168)); bdma_sc1b<2048, 0>(rs, (u32)(wo0), xl_u, (u32)(RS(12) * 7168)); bdma_sc1b<4096, 0>(rs, (u32)(wo0), xl_u, (u32)(RS(13) * 7168)); bdma_sc1b<6144, 0>(rs, (u32)(wo0), xl_u, (u32)(RS(14) * 7168)); bdma_sc1b<32768, 0>(rs, (u32)(wo0), xl_u, (u32)(RS(15) * 7168)); bdma_sc1b<34816, 0>(rs, (u32)(wo0), xl_u, (u32)(RS(16) * 7168)); bdma_sc1b<36864, 0>(rs, (u32)(wo0), xl_u, (u32)(RS(17) * 7168)); bdma_sc1b<38912, 0>(rs, (u32)(wo0), xl_u, (u32)(RS(18) * 7168)); bdma_sc1b<65536, 0>(rs, (u32)(wo0), xl_u, (u32)(RS(19) * 7168)); bdma_sc1b<67584, 0>(rs, (u32)(wo0), xl_u, (u32)(RS(20) * 7168)); bdma_sc1b<69632, 0>(rs, (u32)(wo0), xl_u, (u32)(RS(21) * 7168)); bdma_sc1b<71680, 0>(rs, (u32)(wo0), xl_u, (u32)(RS(22) * 7168)); bdma_sc1b<1024, 1024>(rs, (u32)(wo1x), xl_u, (u32)(RS(11) * 7168)); bdma_sc1b<3072, 1024>(rs, (u32)(wo1x), xl_u, (u32)(RS(12) * 7168)); bdma_sc1b<5120, 1024>(rs, (u32)(wo1x), xl_u, (u32)(RS(13) * 7168)); bdma_sc1b<7168, 1024>(rs, (u32)(wo1x), xl_u, (u32)(RS(14) * 7168)); bdma_sc1b<33792, 1024>(rs, (u32)(wo1x), xl_u, (u32)(RS(15) * 7168)); bdma_sc1b<35840, 1024>(rs, (u32)(wo1x), xl_u, (u32)(RS(16) * 7168)); bdma_sc1b<37888, 1024>(rs, (u32)(wo1x), xl_u, (u32)(RS(17) * 7168)); bdma_sc1b<39936, 1024>(rs, (u32)(wo1x), xl_u, (u32)(RS(18) * 7168)); bdma_sc1b<66560, 1024>(rs, (u32)(wo1x), xl_u, (u32)(RS(19) * 7168)); bdma_sc1b<68608, 1024>(rs, (u32)(wo1x), xl_u, (u32)(RS(20) * 7168)); bdma_sc1b<70656, 1024>(rs, (u32)(wo1x), xl_u, (u32)(RS(21) * 7168)); bdma_sc1b<72704, 1024>(rs, (u32)(wo1x), xl_u, (u32)(RS(22) * 7168)); }
    SB0();
    poll_lanes(rs, OFF_HB + (n2 >> 2) * 128, true, tag, lane);
#define LDH(s) do { hb[s][0] = __builtin_amdgcn_raw_buffer_load_b128(rs, ho0, RS(s) * 4096, AUX_SC1); \
                    hb[s][1] = __builtin_amdgcn_raw_buffer_load_b128(rs, ho0, RS(s) * 4096 + 112 * 1024, AUX_SC1); } while (0)
#pragma unroll
    for (int s = 0; s < 10; ++s) LDH(s);
    SB0();
    f32x16 bc[2][2];
    bf16x8 la[2][2];
#pragma unroll
    for (int a = 0; a < 4; ++a)
#pragma unroll
      for (int i = 0; i < 16; ++i) bc[a >> 1][a & 1][i] = 0.f;
    bc[0][0] = mfma32(as_frag(wdv[0][0]), as_frag(hb[0][0]), bc[0][0]); bc[0][1] = mfma32(as_frag(wdv[0][0]), as_frag(hb[0][1]), bc[0][1]); bc[1][0] = mfma32(as_frag(wdv[0][1]), as_frag(hb[0][0]), bc[1][0]); bc[1][1] = mfma32(as_frag(wdv[0][1]), as_frag(hb[0][1]), bc[1][1]); SB0();
    bc[0][0] = mfma32(as_frag(wdv[1][0]), as_frag(hb[1][0]), bc[0][0]); bc[0][1] = mfma32(as_frag(wdv[1][0]), as_frag(hb[1][1]), bc[0][1]); bc[1][0] = mfma32(as_frag(wdv[1][1]), as_frag(hb[1][0]), bc[1][0]); bc[1][1] = mfma32(as_frag(wdv[1][1]), as_frag(hb[1][1]), bc[1][1]); SB0();
    bc[0][0] = mfma32(as_frag(wdv[2][0]), as_frag(hb[2][0]), bc[0][0]); bc[0][1] = mfma32(as_frag(wdv[2][0]), as_frag(hb[2][1]), bc[0][1]); bc[1][0] = mfma32(as_frag(wdv[2][1]), as_frag(hb[2][0]), bc[1][0]); bc[1][1] = mfma32(as_frag(wdv[2][1]), as_frag(hb[2][1]), bc[1][1]); SB0();
    bc[0][0] = mfma32(as_frag(wdv[3][0]), as_frag(hb[3][0]), bc[0][0]); bc[0][1] = mfma32(as_frag(wdv[3][0]), as_frag(hb[3][1]), bc[0][1]); bc[1][0] = mfma32(as_frag(wdv[3][1]), as_frag(hb[3][0]), bc[1][0]); bc[1][1] = mfma32(as_frag(wdv[3][1]), as_frag(hb[3][1]), bc[1][1]); SB0();
    lds_wait1(lds3, C_RARR, 1u);
    LDH(10); LDH(11); LDH(12); LDH(13); LDH(14); LDH(15); LDH(16); LDH(17); SB0();
    bc[0][0] = mfma32(as_frag(wdv[4][0]), as_frag(hb[4][0]), bc[0][0]); bc[0][1] = mfma32(as_frag(wdv[4][0]), as_frag(hb[4][1]), bc[0][1]); bc[1][0] = mfma32(as_frag(wdv[4][1]), as_frag(hb[4][0]), bc[1][0]); bc[1][1] = mfma32(as_frag(wdv[4][1]), as_frag(hb[4][1]), bc[1][1]); SB0(); LDH(18); LDH(19); SB0();
    bc[0][0] = mfma32(as_frag(wdv[5][0]), as_frag(hb[5][0]), bc[0][0]); bc[0][1] = mfma32(as_frag(wdv[5][0]), as_frag(hb[5][1]), bc[0][1]); bc[1][0] = mfma32(as_frag(wdv[5][1]), as_frag(hb[5][0]), bc[1][0]); bc[1][1] = mfma32(as_frag(wdv[5][1]), as_frag(hb[5][1]), bc[1][1]); SB0(); LDH(20); LDH(21); SB0();
    bc[0][0] = mfma32(as_frag(wdv[6][0]), as_frag(hb[6][0]), bc[0][0]); bc[0][1] = mfma32(as_frag(wdv[6][0]), as_frag(hb[6][1]), bc[0][1]); bc[1][0] = mfma32(as_frag(wdv[6][1]), as_frag(hb[6][0]), bc[1][0]); bc[1][1] = mfma32(as_frag(wdv[6][1]), as_frag(hb[6][1]), bc[1][1]); SB0(); LDH(22); LDH(23); SB0();
    bc[0][0] = mfma32(as_frag(wdv[7][0]), as_frag(hb[7][0]), bc[0][0]); bc[0][1] = mfma32(as_frag(wdv[7][0]), as_frag(hb[7][1]), bc[0][1]); bc[1][0] = mfma32(as_frag(wdv[7][1]), as_frag(hb[7][0]), bc[1][0]); bc[1][1] = mfma32(as_frag(wdv[7][1]), as_frag(hb[7][1]), bc[1][1]); SB0(); LDH(24); LDH(25); SB0();
    bc[0][0] = mfma32(as_frag(wdv[8][0]), as_frag(hb[8][0]), bc[0][0]); bc[0][1] = mfma32(as_frag(wdv[8][0]), as_frag(hb[8][1]), bc[0][1]); bc[1][0] = mfma32(as_frag(wdv[8][1]), as_frag(hb[8][0]), bc[1][0]); bc[1][1] = mfma32(as_frag(wdv[8][1]), as_frag(hb[8][1]), bc[1][1]); SB0(); LDH(26); LDH(27); SB0();
    bc[0][0] = mfma32(as_frag(wdv[9][0]), as_frag(hb[9][0]), bc[0][0]); bc[0][1] = mfma32(as_frag(wdv[9][0]), as_frag(hb[9][1]), bc[0][1]); bc[1][0] = mfma32(as_frag(wdv[9][1]), as_frag(hb[9][0]), bc[1][0]); bc[1][1] = mfma32(as_frag(wdv[9][1]), as_frag(hb[9][1]), bc[1][1]); SB0(); SB0();
    la[1][0] = *(LAS bf16x8*)(xl_p + 0 + lane * 16); la[1][1] = *(LAS bf16x8*)(xl_p + 0 + p1); SB0();
    bc[0][0] = mfma32(as_frag(wdv[10][0]), as_frag(hb[10][0]), bc[0][0]); bc[0][1] = mfma32(as_frag(wdv[10][0]), as_frag(hb[10][1]), bc[0][1]); bc[1][0] = mfma32(as_frag(wdv[10][1]), as_frag(hb[10][0]), bc[1][0]); bc[1][1] = mfma32(as_frag(wdv[10][1]), as_frag(hb[10][1]), bc[1][1]); SB0(); SB0();
    la[0][0] = *(LAS bf16x8*)(xl_p + 2048 + lane * 16); la[0][1] = *(LAS bf16x8*)(xl_p + 2048 + p1); SB0();
    bc[0][0] = mfma32(la[1][0], as_frag(hb[11][0]), bc[0][0]); bc[0][1] = mfma32(la[1][0], as_frag(hb[11][1]), bc[0][1]); bc[1][0] = mfma32(la[1][1], as_frag(hb[11][0]), bc[1][0]); bc[1][1] = mfma32(la[1][1], as_frag(hb[11][1]), bc[1][1]); SB0(); SB0();
    la[1][0] = *(LAS bf16x8*)(xl_p + 4096 + lane * 16); la[1][1] = *(LAS bf16x8*)(xl_p + 4096 + p1); SB0();
    bc[0][0] = mfma32(la[0][0], as_frag(hb[12][0]), bc[0][0]); bc[0][1] = mfma32(la[0][0], as_frag(hb[12][1]), bc[0][1]); bc[1][0] = mfma32(la[0][1], as_frag(hb[12][0]), bc[1][0]); bc[1][1] = mfma32(la[0][1], as_frag(hb[12][1]), bc[1][1]); SB0(); SB0();
    la[0][0] = *(LAS bf16x8*)(xl_p + 6144 + lane * 16); la[0][1] = *(LAS bf16x8*)(xl_p + 6144 + p1); SB0();
    bc[0][0] = mfma32(la[1][0], as_frag(hb[13][0]), bc[0][0]); bc[0][1] = mfma32(la[1][0], as_frag(hb[13][1]), bc[0][1]); bc[1][0] = mfma32(la[1][1], as_frag(hb[13][0]), bc[1][0]); bc[1][1] = mfma32(la[1][1], as_frag(hb[13][1]), bc[1][1]); SB0(); SB0();
    la[1][0] = *(LAS bf16x8*)(xl_p + 32768 + lane * 16); la[1][1] = *(LAS bf16x8*)(xl_p + 32768 + p1); SB0();
    bc[0][0] = mfma32(la[0][0], as_frag(hb[14][0]), bc[0][0]); bc[0][1] = mfma32(la[0][0], as_frag(hb[14][1]), bc[0][1]); bc[1][0] = mfma32(la[0][1], as_frag(hb[14][0]), bc[1][0]); bc[1][1] = mfma32(la[0][1], as_frag(hb[14][1]), bc[1][1]); SB0(); SB0();
    la[0][0] = *(LAS bf16x8*)(xl_p + 34816 + lane * 16); la[0][1] = *(LAS bf16x8*)(xl_p + 34816 + p1); SB0();
    bc[0][0] = mfma32(la[1][0], as_frag(hb[15][0]), bc[0][0]); bc[0][1] = mfma32(la[1][0], as_frag(hb[15][1]), bc[0][1]); bc[1][0] = mfma32(la[1][1], as_frag(hb[15][0]), bc[1][0]); bc[1][1] = mfma32(la[1][1], as_frag(hb[15][1]), bc[1][1]); SB0(); SB0();
    la[1][0] = *(LAS bf16x8*)(xl_p + 36864 + lane * 16); la[1][1] = *(LAS bf16x8*)(xl_p + 36864 + p1); SB0();
    bc[0][0] = mfma32(la[0][0], as_frag(hb[16][0]), bc[0][0]); bc[0][1] = mfma32(la[0][0], as_frag(hb[16][1]), bc[0][1]); bc[1][0] = mfma32(la[0][1], as_frag(hb[16][0]), bc[1][0]); bc[1][1] = mfma32(la[0][1], as_frag(hb[16][1]), bc[1][1]); SB0(); SB0();
    la[0][0] = *(LAS bf16x8*)(xl_p + 38912 + lane * 16); la[0][1] = *(LAS bf16x8*)(xl_p + 38912 + p1); SB0();
    bc[0][0] = mfma32(la[1][0], as_frag(hb[17][0]), bc[0][0]); bc[0][1] = mfma32(la[1][0], as_frag(hb[17][1]), bc[0][1]); bc[1][0] = mfma32(la[1][1], as_frag(hb[17][0]), bc[1][0]); bc[1][1] = mfma32(la[1][1], as_frag(hb[17][1]), bc[1][1]); SB0(); SB0();
    la[1][0] = *(LAS bf16x8*)(xl_p + 65536 + lane * 16); la[1][1] = *(LAS bf16x8*)(xl_p + 65536 + p1); SB0();
    bc[0][0] = mfma32(la[0][0], as_frag(hb[18][0]), bc[0][0]); bc[0][1] = mfma32(la[0][0], as_frag(hb[18][1]), bc[0][1]); bc[1][0] = mfma32(la[0][1], as_frag(hb[18][0]), bc[1][0]); bc[1][1] = mfma32(la[0][1], as_frag(hb[18][1]), bc[1][1]); SB0(); SB0();
    la[0][0] = *(LAS bf16x8*)(xl_p + 67584 + lane * 16); la[0][1] = *(LAS bf16x8*)(xl_p + 67584 + p1); SB0();
    bc[0][0] = mfma32(la[1][0], as_frag(hb[19][0]), bc[0][0]); bc[0][1] = mfma32(la[1][0], as_frag(hb[19][1]), bc[0][1]); bc[1][0] = mfma32(la[1][1], as_frag(hb[19][0]), bc[1][0]); bc[1][1] = mfma32(la[1][1], as_frag(hb[19][1]), bc[1][1]); SB0(); SB0();
    la[1][0] = *(LAS bf16x8*)(xl_p + 69632 + lane * 16); la[1][1] = *(LAS bf16x8*)(xl_p + 69632 + p1); SB0();
    bc[0][0] = mfma32(la[0][0], as_frag(hb[20][0]), bc[0][0]); bc[0][1] = mfma32(la[0][0], as_frag(hb[20][1]), bc[0][1]); bc[1][0] = mfma32(la[0][1], as_frag(hb[20][0]), bc[1][0]); bc[1][1] = mfma32(la[0][1], as_frag(hb[20][1]), bc[1][1]); SB0(); SB0();
    la[0][0] = *(LAS bf16x8*)(xl_p + 71680 + lane * 16); la[0][1] = *(LAS bf16x8*)(xl_p + 71680 + p1); SB0();
    bc[0][0] = mfma32(la[1][0], as_frag(hb[21][0]), bc[0][0]); bc[0][1] = mfma32(la[1][0], as_frag(hb[21][1]), bc[0][1]); bc[1][0] = mfma32(la[1][1], as_frag(hb[21][0]), bc[1][0]); bc[1][1] = mfma32(la[1][1], as_frag(hb[21][1]), bc[1][1]); SB0(); SB0();
    la[1][0] = *(LAS bf16x8*)(wl_p + 0 + lane * 16); la[1][1] = *(LAS bf16x8*)(wl_p + 0 + p1); SB0();
    bc[0][0] = mfma32(la[0][0], as_frag(hb[22][0]), bc[0][0]); bc[0][1] = mfma32(la[0][0], as_frag(hb[22][1]), bc[0][1]); bc[1][0] = mfma32(la[0][1], as_frag(hb[22][0]), bc[1][0]); bc[1][1] = mfma32(la[0][1], as_frag(hb[22][1]), bc[1][1]); SB0(); SB0();
    la[0][0] = *(LAS bf16x8*)(wl_p + 2048 + lane * 16); la[0][1] = *(LAS bf16x8*)(wl_p + 2048 + p1); SB0();
    bc[0][0] = mfma32(la[1][0], as_frag(hb[23][0]), bc[0][0]); bc[0][1] = mfma32(la[1][0], as_frag(hb[23][1]), bc[0][1]); bc[1][0] = mfma32(la[1][1], as_frag(hb[23][0]), bc[1][0]); bc[1][1] = mfma32(la[1][1], as_frag(hb[23][1]), bc[1][1]); SB0(); SB0();
    la[1][0] = *(LAS bf16x8*)(wl_p + 4096 + lane * 16); la[1][1] = *(LAS bf16x8*)(wl_p + 4096 + p1); SB0();
    bc[0][0] = mfma32(la[0][0], as_frag(hb[24][0]), bc[0][0]); bc[0][1] = mfma32(la[0][0], as_frag(hb[24][1]), bc[0][1]); bc[1][0] = mfma32(la[0][1], as_frag(hb[24][0]), bc[1][0]); bc[1][1] = mfma32(la[0][1], as_frag(hb[24][1]), bc[1][1]); SB0(); SB0();
    la[0][0] = *(LAS bf16x8*)(wl_p + 6144 + lane * 16); la[0][1] = *(LAS bf16x8*)(wl_p + 6144 + p1); SB0();
    bc[0][0] = mfma32(la[1][0], as_frag(hb[25][0]), bc[0][0]); bc[0][1] = mfma32(la[1][0], as_frag(hb[25][1]), bc[0][1]); bc[1][0] = mfma32(la[1][1], as_frag(hb[25][0]), bc[1][0]); bc[1][1] = mfma32(la[1][1], as_frag(hb[25][1]), bc[1][1]); SB0(); SB0();
    la[1][0] = *(LAS bf16x8*)(wl_p + 8192 + lane * 16); la[1][1] = *(LAS bf16x8*)(wl_p + 8192 + p1); SB0();
    bc[0][0] = mfma32(la[0][0], as_frag(hb[26][0]), bc[0][0]); bc[0][1] = mfma32(la[0][0], as_frag(hb[26][1]), bc[0][1]); bc[1][0] = mfma32(la[0][1], as_frag(hb[26][0]), bc[1][0]); bc[1][1] = mfma32(la[0][1], as_frag(hb[26][1]), bc[1][1]); SB0(); SB0();
    bc[0][0] = mfma32(la[1][0], as_frag(hb[27][0]), bc[0][0]); bc[0][1] = mfma32(la[1][0], as_frag(hb[27][1]), bc[0][1]); bc[1][0] = mfma32(la[1][1], as_frag(hb[27][0]), bc[1][0]); bc[1][1] = mfma32(la[1][1], as_frag(hb[27][1]), bc[1][1]); SB0(); SB0();
#undef LDH
    WFENCE();
#pragma unroll
    for (int t = 0; t < 2; ++t)
#pragma unroll
      for (int st = 0; st < 2; ++st)
#pragma unroll
        for (int qd = 0; qd < 4; ++qd)
          *(LAS f32x4*)(xl_p + t * 32768 + (st * 4 + qd) * 1024 + lane * 16) = f32x4{bc[t][st][4 * qd], bc[t][st][4 * qd + 1], bc[t][st][4 * qd + 2], bc[t][st][4 * qd + 3]};
    lds_set(lds3, C_B + wl, 1u, lane);
  } else {
    const int item = xcd * 29 + idx;
    const bool has_item = idx < 29 && item < 231;
    const int itc = has_item ? item : 0;
    if (wl == 2 && gemm) {
      const __attribute__((address_space(1))) char* pc0 = (const __attribute__((address_space(1))) char*)((u64)pc_start & ~(u64)255);
      u32x4 t[6];
#pragma unroll
      for (int i = 0; i < 6; ++i) t[i] = *(const __attribute__((address_space(1))) u32x4*)(pc0 + ((idx & 7) * 6 + i) * 1024 * CODE_KB / 48 / 16 * 16 + lane * 16);
      u32x4 t6 = *(const u32x4*)((const char*)&gT3 + (idx & 15) * 1024 + lane * 16);
      u32x4 t7 = *(const u32x4*)((const char*)&gTw2048 + (idx & 15) * 1024 + lane * 16);
      asm volatile("" :: "v"(t[0]), "v"(t[1]), "v"(t[2]), "v"(t[3]), "v"(t[4]), "v"(t[5]), "v"(t6), "v"(t7));
    }
    if (gemm) {
      if (wl == 0) { u32x4 rw[8]; prep_wd_issue(rw, wd, b, lane); prep_wd_finish(rw, rs, b, lds3 + L_F, lane); }
      else if (wl == 1) { u32 a224, b224; prep_filter<true, true>(w_r, w_i, rs, b, lds3 + L_F + 7296, lane, a224, b224); }
      if (wl == 2) put_tag(lds3, ld64s(rs, OFF_E), 7, lane);
      DRAIN();
    }
    const u32x2 tag = get_tag(lds3);
    if (lds_arrive(lds3, C_QCNT, lane) == 3u) { if (gemm && lane < 8) st64s(rs, OFF_QF + lane * 2048 + b * 8, tag); }
    FftPre fp;
    cf v[8];
    fft_prefetch_static(fp, w_r, w_i, itc, wl, lane);
    SB0();
    SB0();
    if (wl == 0) {
      if (gemm) lds_wait4(lds3, C_W, FSTART);
      poll_lanes(rs, OFF_PF + xcd * 256 + (lane & 31) * 8, true, tag, lane);
      lds_set(lds3, C_PGO, 1u, lane);
    } else {
      lds_wait1(lds3, C_PGO, 1u);
    }
    if (has_item) {
      fft_loadP(v, rs, itc, wl, lane);
      const u32x4 qa = ld128s(rs, OFF_QF + xcd * 2048 + lane * 16), qb = ld128s(rs, OFF_QF + xcd * 2048 + 1024 + lane * 16);
      SB0();
      __builtin_amdgcn_s_setprio(FPRIO);
      fft_item(rs, fp, v, item, lds3, wl, lane, qa, qb, tag, OFF_QF + xcd * 2048);
      __builtin_amdgcn_s_setprio(0);
      DRAIN();
    }
    if (!has_item) {
      if (wl == 0) { poll256(rs, OFF_QF + xcd * 2048, tag, lane); lds_set(lds3, C_QGO, 1u, lane); }
      (void)lds_arrive(lds3, C_FIN, lane);
    }
    if (lds_arrive(lds3, C_RCNT, lane) == 3u) {
      if (has_item && glb_arrive(ws, 8 + xcd, lane) == (xcd == 7 ? 27u : 28u) && glb_arrive(ws, 16, lane) == 7u) { if (lane < 8) st64s(rs, OFF_RB + lane * 128, tag); }
      lds_set(lds3, C_RARR, 1u, lane);
    }
    bf16x8 bt[4][2];
#pragma unroll
    for (int ks = 0; ks < 4; ++ks)
#pragma unroll
      for (int hl2 = 0; hl2 < 2; ++hl2) bt[ks][hl2] = *(const bf16x8*)&gT3.v[wl & 1][ks][hl2][lane][0];
    SB0();
    if (wl == 0) {
      poll_lanes(rs, lane == 0 ? OFF_RB + xcd * 128 : OFF_GF + n2 * 128, true, tag, lane);
      lds_set(lds3, C_RGO, 1u, lane);
    } else {
      lds_wait1(lds3, C_RGO, 1u);
    }
    const float gate = __builtin_bit_cast(float, ld32s(rs, WS_GATE + (n2 * 64 + 32 * (wl & 1) + (lane & 31)) * 4));
    final_phase(rs, out, n2, j, lds3, wl, lane, bt, gate);
    if (b == 0 && wl == 0 && lane == 0) st64s(rs, OFF_E, tag);
  }
}

extern "C" void kernel_launch(void* const* d_in, const int* in_sizes, int n_in, void* d_out, int out_size,
                              void* d_ws, size_t ws_size, hipStream_t stream) {
  (void)hipFuncSetAttribute((const void*)mega_kernel, hipFuncAttributeMaxDynamicSharedMemorySize, LDS_BYTES);
  mega_kernel<<<256, 512, LDS_BYTES, stream>>>((const u16*)d_in[0], (const u16*)d_in[1], (const u16*)d_in[2],
                                               (const u16*)d_in[4], (const u16*)d_in[5], (const u16*)d_in[6],
                                               (const u16*)d_in[7], (char*)d_ws, (u16*)d_out);
}
```
